```python
import torch

def _softmax(x, axis=-1):
    return torch.softmax(x, dim=axis)

def _gelu(x):
    return torch.nn.functional.gelu(x, approximate='tanh')

def _silu(x):
    return torch.nn.functional.silu(x)

def _sigmoid(x):
    return torch.sigmoid(x)

def _layernorm(x, axis=-1, eps=1e-05):
    m = x.mean(dim=axis, keepdim=True)
    v = x.var(dim=axis, keepdim=True, unbiased=False)
    return (x - m) / torch.sqrt(v + eps)

def _scatter_add(tgt, idx, src, axis=0):
    return tgt.clone().index_add_(axis, idx, src)

def _segment_sum(src, seg, n):
    out = torch.zeros((n,) + src.shape[1:], dtype=src.dtype)
    return out.index_add_(0, seg, src)

def _pad(x, pw):
    flat = tuple((v for pair in reversed(pw) for v in pair))
    return torch.nn.functional.pad(x, flat)
kernel_name = 'recurrent_scan__sliding_block__mixer_gmlp__prompt'

def setup_inputs():

    _w = lambda a: (lambda t: t.to(torch.bfloat16) if t.is_floating_point() else t)(torch.as_tensor(a))

    def _A_setup():
        B, S, D = (1, 2048, 576)
        return {'x': _w((lambda *s: torch.randn(*s, dtype=torch.bfloat16) * 0.05)(B, S, D)), 'wqkv': _w((lambda *s: torch.randn(*s, dtype=torch.bfloat16) * 0.05)(D, 3 * D)), 'wo': _w((lambda *s: torch.randn(*s, dtype=torch.bfloat16) * 0.05)(D, D))}
    _A_in = _A_setup()

    def _B_setup():
        B, S, D, F = (1, 2048, 576, 4608)
        Fh = F // 2
        return {'x': _w((lambda *s: torch.randn(*s, dtype=torch.bfloat16) * 0.05)(B, S, D)), 'w_in': _w((lambda *s: torch.randn(*s, dtype=torch.bfloat16) * 0.4)(D, 2 * Fh)), 'w_sp': _w(0.01 * (lambda *s: torch.randn(*s, dtype=torch.bfloat16) * 0.4)(S, S)), 'b_sp': _w(1.0 + 0.01 * (lambda *s: torch.randn(*s, dtype=torch.bfloat16) * 0.05)(S)), 'w_out': _w((lambda *s: torch.randn(*s, dtype=torch.bfloat16) * 0.05)(Fh, D))}
    _B_in = _B_setup()
    return {'A_x': _A_in['x'], 'A_wqkv': _A_in['wqkv'], 'A_wo': _A_in['wo'], 'B_x': _B_in['x'], 'B_w_in': _B_in['w_in'], 'B_w_sp': _B_in['w_sp'], 'B_b_sp': _B_in['b_sp'], 'B_w_out': _B_in['w_out']}

def reference(A_x, A_wqkv, A_wo, B_x, B_w_in, B_w_sp, B_b_sp, B_w_out):

    def _A_ref(x, wqkv, wo):
        B, S, D = x.shape
        H = 32
        Dh = D // H
        W = 256
        qkv = (lambda x, *d: x.permute(*d))((x @ wqkv).reshape(B, S, 3, H, Dh), 2, 0, 3, 1, 4)
        q, k, v = (qkv[0], qkv[1], qkv[2])
        scores = torch.einsum('bhqd,bhkd->bhqk', q, k) * Dh ** (-0.5)
        idx = torch.arange(S)
        mask = (idx[:, None] - idx[None, :] >= 0) & (idx[:, None] - idx[None, :] < W)
        scores = torch.where(mask[None, None], scores, -1000000000.0)
        p = _softmax(scores, axis=-1)
        o = torch.einsum('bhqk,bhkd->bhqd', p, v)
        return (lambda x, *d: x.permute(*d))(o, 0, 2, 1, 3).reshape(B, S, D) @ wo

    def _B_ref(x, w_in, w_sp, b_sp, w_out):
        B, S, D = x.shape
        F = w_in.shape[-1]
        Fh = F // 2
        z = _gelu(torch.einsum('bsd,df->bsf', x, w_in))
        z1 = z[..., :Fh].clone()
        z2 = _layernorm(z[..., Fh:], axis=-1)
        g = torch.einsum('ts,bsf->btf', w_sp, z2) + b_sp[None, :, None]
        return torch.einsum('bsf,fd->bsd', z1 * g, w_out)
    x = A_x
    xn = _layernorm(x, axis=-1)
    s = _A_ref(xn, A_wqkv, A_wo)
    g = _silu(_B_ref(xn, B_w_in, B_w_sp, B_b_sp, B_w_out))
    return x + s * g
```

```cpp
#include <hip/hip_runtime.h>
#include <hip/hip_bf16.h>
#include <stdint.h>

typedef __attribute__((ext_vector_type(8))) __bf16 bf16x8;
typedef __attribute__((ext_vector_type(4))) __bf16 bf16x4;
typedef __attribute__((ext_vector_type(2))) __bf16 bf16x2;
typedef __attribute__((ext_vector_type(4))) float f32x4;
typedef __attribute__((ext_vector_type(2))) float f32x2;
typedef __attribute__((ext_vector_type(4))) unsigned u32x4;
typedef __attribute__((ext_vector_type(2))) unsigned u32x2;
typedef u32x4 u32x4_a4 __attribute__((aligned(4)));
typedef u32x2 u32x2_a4 __attribute__((aligned(4)));
#define AS1 __attribute__((address_space(1)))
#define AS3 __attribute__((address_space(3)))
#define DEVI __device__ __forceinline__

constexpr int S_ = 2048, D_ = 576, FH = 2304;

constexpr size_t WS_XN   = 0;
constexpr size_t WS_Z1   = WS_XN + (size_t)S_ * D_ * 2;
constexpr size_t WS_Z2   = WS_Z1 + (size_t)S_ * FH * 2;
constexpr size_t WS_H    = WS_Z2 + (size_t)S_ * FH * 2;
constexpr size_t WS_QKV  = WS_H + (size_t)S_ * FH * 2;
constexpr size_t WS_O    = WS_QKV + (size_t)96 * S_ * 18 * 2;
constexpr size_t WS_PART = WS_O + (size_t)S_ * D_ * 2;
constexpr size_t WS_STAT = WS_PART + (size_t)4 * S_ * D_ * 4;

DEVI void glds16(const char* g, char* lds) {
  __builtin_amdgcn_global_load_lds((const AS1 void*)g, (AS3 void*)lds, 16, 0, 0);
}
DEVI bf16x8 lds_b128(const char* p) { return *(const AS3 bf16x8*)p; }
DEVI bf16x4 lds_tr(const char* p) { return __builtin_amdgcn_ds_read_tr16_b64_v4bf16((AS3 bf16x4*)p); }
DEVI bf16x8 cat8(bf16x4 a, bf16x4 b) { return __builtin_shufflevector(a, b, 0, 1, 2, 3, 4, 5, 6, 7); }
DEVI f32x4 mfma(bf16x8 a, bf16x8 b, f32x4 c) { return __builtin_amdgcn_mfma_f32_16x16x32_bf16(a, b, c, 0, 0, 0); }
DEVI unsigned pk2(float a, float b) {
  f32x2 v = {a, b};
  bf16x2 r = __builtin_convertvector(v, bf16x2);
  return __builtin_bit_cast(unsigned, r);
}
DEVI float bflo(unsigned u) { return __builtin_bit_cast(float, u << 16); }
DEVI float bfhi(unsigned u) { return __builtin_bit_cast(float, u & 0xffff0000u); }
template <int N> DEVI void wait_vm_lgkm0() { asm volatile("s_waitcnt vmcnt(%0) lgkmcnt(0)" ::"n"(N) : "memory"); }
DEVI void bar() { __builtin_amdgcn_s_barrier(); asm volatile("" ::: "memory"); }
DEVI void lds_bar() { asm volatile("s_waitcnt lgkmcnt(0)" ::: "memory"); bar(); }
DEVI float gelu_t(float y) {
  float p = __builtin_fmaf(y * y, -0.1029432397f, -2.3022081983f);
  float e = __builtin_amdgcn_exp2f(y * p);
  return y * __builtin_amdgcn_rcpf(1.0f + e);
}
DEVI int swap23(int r) { return (r & ~12) | ((r & 4) << 1) | ((r & 8) >> 1); }

__global__ __launch_bounds__(64) void k0_ln(const char* __restrict__ x, char* __restrict__ ws) {
  const int l = threadIdx.x, b = blockIdx.x;
  const int row = b * 8 + (l >> 3), c0 = l & 7;
  const char* src = x + (size_t)row * 1152 + c0 * 16;
  u32x4 v[9];
#pragma unroll
  for (int i = 0; i < 9; i++) v[i] = *(const u32x4*)(src + i * 128);
  *(u32x4*)(ws + WS_STAT + (size_t)b * 1024 + l * 16) = (u32x4){~0u, ~0u, ~0u, ~0u};
  float s = 0.f;
#pragma unroll
  for (int i = 0; i < 9; i++)
#pragma unroll
    for (int e = 0; e < 4; e++) s += bflo(v[i][e]) + bfhi(v[i][e]);
  s += __shfl_xor(s, 1); s += __shfl_xor(s, 2); s += __shfl_xor(s, 4);
  const float mean = s * (1.0f / 576.0f);
  float q = 0.f;
#pragma unroll
  for (int i = 0; i < 9; i++)
#pragma unroll
    for (int e = 0; e < 4; e++) {
      float a = bflo(v[i][e]) - mean, c = bfhi(v[i][e]) - mean;
      q += a * a + c * c;
    }
  q += __shfl_xor(q, 1); q += __shfl_xor(q, 2); q += __shfl_xor(q, 4);
  const float rs = 1.0f / __builtin_sqrtf(q * (1.0f / 576.0f) + 1e-5f);
  char* dst = ws + WS_XN + (size_t)row * 1152 + c0 * 16;
#pragma unroll
  for (int i = 0; i < 9; i++) {
    u32x4 o;
#pragma unroll
    for (int e = 0; e < 4; e++) o[e] = pk2((bflo(v[i][e]) - mean) * rs, (bfhi(v[i][e]) - mean) * rs);
    *(u32x4*)(dst + i * 128) = o;
  }
}

constexpr int K1_SLOT = 8192 + 25600;
constexpr int K1_STAT = 4 * K1_SLOT;
constexpr int K1_STAT2 = K1_STAT + 4096;
constexpr int K1_LDS = K1_STAT2 + 4096;

template <int NJ> struct Frag1 { bf16x8 a[NJ]; bf16x8 b[4]; };

DEVI void k1_sub(int J, int& ty, int& ix) {
  if (J < 7)       { if (J < 3) { ty = 0; ix = J; } else if (J < 5) { ty = 1; ix = J - 3; } else { ty = 2; ix = J - 5; } }
  else if (J < 13) { int j = J - 7;  if (j < 2) { ty = 0; ix = 3 + j; } else if (j < 5) { ty = 1; ix = j; } else { ty = 2; ix = 2; } }
  else if (J < 19) { int j = J - 13; if (j < 2) { ty = 0; ix = 5 + j; } else if (j < 4) { ty = 1; ix = 3 + j; } else { ty = 2; ix = j - 1; } }
  else             { int j = J - 19; if (j < 2) { ty = 0; ix = 7 + j; } else if (j < 4) { ty = 1; ix = 5 + j; } else { ty = 2; ix = j + 1; } }
}

template <int NQ>
DEVI void k1_body(char* lds, const char* __restrict__ w_in, const char* __restrict__ wqkv, char* __restrict__ ws,
                  int rb, int cg, int w, int sh) {
  constexpr int NJ = (NQ == 0) ? 7 : 6;
  constexpr int J0 = (NQ == 0) ? 0 : (NQ == 1) ? 7 : (NQ == 2) ? 13 : 19;
  constexpr int NZ2 = (NQ == 0) ? 3 : 2;
  constexpr int NZ1 = (NQ == 1) ? 3 : 2;
  constexpr int Z2I = (NQ == 0) ? 0 : (NQ == 1) ? 3 : (NQ == 2) ? 5 : 7;
  constexpr int Z1I = (NQ == 0) ? 0 : (NQ == 1) ? 2 : (NQ == 2) ? 5 : 7;
  constexpr int QI  = (NQ == 0) ? 0 : (NQ == 1) ? 2 : (NQ == 2) ? 3 : 5;
  constexpr int NS = 18;
  const int tid = threadIdx.x, l = tid & 63, g = l >> 4, c16 = l & 15;
  const int qstart = 108 * cg - ((cg & 1) ? 4 : 0);

  const char* xsrc;
  {
    int r = 16 * w + (l >> 2);
    int c = (l & 3) ^ ((-(r >> 2)) & 3);
    xsrc = ws + WS_XN + (size_t)(rb * 128 + r) * 1152 + c * 16;
  }
  const char* wsrc[4];
  unsigned winc[4];
#pragma unroll
  for (int i = 0; i < 4; i++) {
    int q = w * 200 + i * 64 + l;
    if (i == 3) q = w * 200 + 192 + (l & 7);
    int rho = q / 50, c = q - rho * 50;
    int k = swap23(rho);
    int ty, ix;
    k1_sub(c >> 1, ty, ix);
    int col = ix * 16 + (c & 1) * 8;
    if (ty == 2) { wsrc[i] = wqkv + (size_t)k * 3456 + (qstart + col) * 2; winc[i] = 32 * 3456; }
    else { wsrc[i] = w_in + (size_t)k * 9216 + ((ty == 0 ? FH : 0) + 144 * cg + col) * 2; winc[i] = 32 * 9216; }
  }
  auto issue = [&](int t) {
    char* slot = lds + (t & 3) * K1_SLOT;
    glds16(xsrc + t * 64, slot + w * 1024);
    char* wd = slot + 8192 + w * 3200;
#pragma unroll
    for (int i = 0; i < 3; i++) glds16(wsrc[i] + (size_t)winc[i] * (unsigned)t, wd + i * 1024);
    if (l < 8) glds16(wsrc[3] + (size_t)winc[3] * (unsigned)t, wd + 3072);
  };
  const int boff = (64 * sh + c16) * 64 + ((g ^ ((-(c16 >> 2)) & 3)) * 16);
  const int aoff = 8192 + (16 * (g >> 1) + 4 * (g & 1) + (c16 >> 2)) * 800 + J0 * 32 + 8 * (c16 & 3);
  auto rd = [&](Frag1<NJ>& f, int t) {
    const char* slot = lds + (t & 3) * K1_SLOT;
#pragma unroll
    for (int m = 0; m < 4; m++) f.b[m] = lds_b128(slot + boff + m * 1024);
#pragma unroll
    for (int j = 0; j < NJ; j++)
      f.a[j] = cat8(lds_tr(slot + aoff + j * 32), lds_tr(slot + aoff + j * 32 + 8 * 800));
  };
  f32x4 acc[NJ][4];
#pragma unroll
  for (int j = 0; j < NJ; j++)
#pragma unroll
    for (int m = 0; m < 4; m++) acc[j][m] = (f32x4){0.f, 0.f, 0.f, 0.f};
  auto mm = [&](const Frag1<NJ>& f) {
#pragma unroll
    for (int j = 0; j < NJ; j++)
#pragma unroll
      for (int m = 0; m < 4; m++) acc[j][m] = mfma(f.a[j], f.b[m], acc[j][m]);
  };
  Frag1<NJ> f0, f1;
  issue(0); issue(1); issue(2); issue(3);
  wait_vm_lgkm0<15>();
  bar();
  rd(f0, 0);
#pragma unroll 1
  for (int t = 0; t < NS - 4; t += 2) {
    wait_vm_lgkm0<10>(); bar();
    issue(t + 4); rd(f1, t + 1); mm(f0);
    wait_vm_lgkm0<10>(); bar();
    issue(t + 5); rd(f0, t + 2); mm(f1);
  }
  wait_vm_lgkm0<10>(); bar(); rd(f1, NS - 3); mm(f0);
  wait_vm_lgkm0<5>();  bar(); rd(f0, NS - 2); mm(f1);
  wait_vm_lgkm0<0>();  bar(); rd(f1, NS - 1); mm(f0);
  mm(f1);

  const int srow = rb * 128 + 64 * sh + c16;
  {
    float s1[4], s2[4];
#pragma unroll
    for (int m = 0; m < 4; m++) {
      float a = 0.f, q = 0.f;
#pragma unroll
      for (int j = 0; j < NZ2; j++)
#pragma unroll
        for (int r = 0; r < 4; r++) {
          float z = gelu_t(acc[j][m][r]);
          acc[j][m][r] = z;
          a += z; q = __builtin_fmaf(z, z, q);
        }
      a += __shfl_xor(a, 16); q += __shfl_xor(q, 16);
      a += __shfl_xor(a, 32); q += __shfl_xor(q, 32);
      s1[m] = a; s2[m] = q;
    }
    f32x2* st = (f32x2*)(lds + K1_STAT) + NQ * 128 + 64 * sh + c16;
    if (g == 0) {
#pragma unroll
      for (int m = 0; m < 4; m++) st[16 * m] = (f32x2){s1[m], s2[m]};
    }
  }
  lds_bar();
  AS1 unsigned long long* gstat = (AS1 unsigned long long*)(ws + WS_STAT) + (size_t)rb * 16 * 128;
  if (tid < 128) {
    const f32x2* st = (const f32x2*)(lds + K1_STAT) + tid;
    f32x2 v = st[0] + st[128] + st[256] + st[384];
    const float v0 = v[0], v1 = v[1];
    unsigned lo = __builtin_bit_cast(unsigned, v0), hi = __builtin_bit_cast(unsigned, v1);
    if (hi == 0xffffffffu) hi = 0x7fc00000u;
    __hip_atomic_store(gstat + cg * 128 + tid, ((unsigned long long)hi << 32) | lo, __ATOMIC_RELAXED,
                       __HIP_MEMORY_SCOPE_AGENT);
  }
#pragma unroll
  for (int j = 0; j < NZ1; j++)
#pragma unroll
    for (int m = 0; m < 4; m++) {
      f32x4 v = acc[NZ2 + j][m];
      u32x2 o = {pk2(gelu_t(v[0]), gelu_t(v[1])), pk2(gelu_t(v[2]), gelu_t(v[3]))};
      *(u32x2*)(ws + WS_Z1 + (size_t)(srow + 16 * m) * 4608 + (144 * cg + (Z1I + j) * 16 + 4 * g) * 2) = o;
    }
#pragma unroll
  for (int j = NZ2 + NZ1; j < NJ; j++) {
    const int cl = (QI + (j - NZ2 - NZ1)) * 16 + 4 * g + qstart;
#pragma unroll
    for (int p = 0; p < 2; p++) {
      const int c = cl + 2 * p;
      const bool ok = (c >= 108 * cg) && (c < 108 * cg + 108);
      const int hp = c / 18, d = c - hp * 18;
      char* dst = ws + WS_QKV + ((size_t)hp * S_ + srow) * 36 + d * 2;
      if (ok) {
#pragma unroll
        for (int m = 0; m < 4; m++) *(unsigned*)(dst + m * 16 * 36) = pk2(acc[j][m][2 * p], acc[j][m][2 * p + 1]);
      }
    }
  }
  {
    const int row = tid & 127, part = tid >> 7;
    float a = 0.f, q = 0.f;
#pragma unroll
    for (int i = 0; i < 4; i++) {
      unsigned long long v;
      do {
        v = __hip_atomic_load(gstat + (part * 4 + i) * 128 + row, __ATOMIC_RELAXED, __HIP_MEMORY_SCOPE_AGENT);
      } while ((unsigned)(v >> 32) == 0xffffffffu);
      a += __builtin_bit_cast(float, (unsigned)v);
      q += __builtin_bit_cast(float, (unsigned)(v >> 32));
    }
    ((f32x2*)(lds + K1_STAT2))[part * 128 + row] = (f32x2){a, q};
  }
  lds_bar();
#pragma unroll
  for (int m = 0; m < 4; m++) {
    const f32x2* st = (const f32x2*)(lds + K1_STAT2) + 64 * sh + 16 * m + c16;
    f32x2 v = st[0] + st[128] + st[256] + st[384];
    const float mean = v[0] * (1.0f / FH);
    const float var = fmaxf(v[1] * (1.0f / FH) - mean * mean, 0.f);
    const float rs = 1.0f / __builtin_sqrtf(var + 1e-5f);
#pragma unroll
    for (int j = 0; j < NZ2; j++) {
      f32x4 z = acc[j][m];
      u32x2 o = {pk2((z[0] - mean) * rs, (z[1] - mean) * rs), pk2((z[2] - mean) * rs, (z[3] - mean) * rs)};
      *(u32x2*)(ws + WS_Z2 + (size_t)(srow + 16 * m) * 4608 + (144 * cg + (Z2I + j) * 16 + 4 * g) * 2) = o;
    }
  }
}

__global__ __launch_bounds__(512, 2) void k1_proj(const char* __restrict__ w_in, const char* __restrict__ wqkv,
                                                  char* __restrict__ ws) {
  extern __shared__ __attribute__((aligned(16))) char lds[];
  const int b = blockIdx.x, xcd = b & 7, i = b >> 3;
  const int rb = 8 * (xcd & 1) + (i & 7), cg = 4 * (xcd >> 1) + (i >> 3);
  const int w = __builtin_amdgcn_readfirstlane(threadIdx.x >> 6);
  const int sh = w >> 2, nq = sh ? 3 - (w & 3) : (w & 3);
  if (nq == 0) k1_body<0>(lds, w_in, wqkv, ws, rb, cg, w, sh);
  else if (nq == 1) k1_body<1>(lds, w_in, wqkv, ws, rb, cg, w, sh);
  else if (nq == 2) k1_body<2>(lds, w_in, wqkv, ws, rb, cg, w, sh);
  else k1_body<3>(lds, w_in, wqkv, ws, rb, cg, w, sh);
}

constexpr int AT_LDS = 65536;
__global__ __launch_bounds__(512, 2) void k_attn(char* __restrict__ ws) {
  extern __shared__ __attribute__((aligned(16))) char lds[];
  const int b = blockIdx.x, h = b & 31, qb = b >> 5;
  const int tid = threadIdx.x, l = tid & 63, g = l >> 4, c16 = l & 15;
  const int w = __builtin_amdgcn_readfirstlane(tid >> 6);
  const int q0 = qb * 256;
  const char* qkv = ws + WS_QKV;
  {
    const int key = q0 - 256 + tid;
    u32x4 k0 = {0, 0, 0, 0}, k1 = k0, v0 = k0, v1 = k0;
    unsigned k2 = 0, v2 = 0;
    if (key >= 0) {
      const char* kp = qkv + ((size_t)(32 + h) * S_ + key) * 36;
      const char* vp = qkv + ((size_t)(64 + h) * S_ + key) * 36;
      k0 = *(const u32x4_a4*)kp; k1 = *(const u32x4_a4*)(kp + 16); k2 = *(const unsigned*)(kp + 32);
      v0 = *(const u32x4_a4*)vp; v1 = *(const u32x4_a4*)(vp + 16); v2 = *(const unsigned*)(vp + 32);
    }
    const int f = (-(tid >> 2)) & 3;
    char* kr = lds + tid * 64;
    *(u32x4*)(kr + ((0 ^ f) * 16)) = k0;
    *(u32x4*)(kr + ((1 ^ f) * 16)) = k1;
    *(u32x4*)(kr + ((2 ^ f) * 16)) = (u32x4){k2, 0, 0, 0};
    *(u32x4*)(kr + ((3 ^ f) * 16)) = (u32x4){0, 0, 0, 0};
    char* vr = lds + 32768 + tid * 64;
    const int x = ((tid >> 2) & 1) * 32;
    *(u32x4*)(vr + (0 ^ x)) = v0;
    *(u32x4*)(vr + (16 ^ x)) = v1;
    *(u32x4*)(vr + (32 ^ x)) = (u32x4){v2, 0x00003f80u, 0, 0};
    *(u32x4*)(vr + (48 ^ x)) = (u32x4){0, 0, 0, 0};
  }
  bf16x8 qf[2];
#pragma unroll
  for (int m = 0; m < 2; m++) {
    const char* qp = qkv + ((size_t)h * S_ + q0 + 32 * w + 16 * m + c16) * 36;
    u32x4 v = {0, 0, 0, 0};
    if (g < 2) v = *(const u32x4_a4*)(qp + 16 * g);
    else if (g == 2) v[0] = *(const unsigned*)(qp + 32);
    qf[m] = __builtin_bit_cast(bf16x8, v);
  }
  __syncthreads();
  const float sc = 0.23570226039551584f * 1.4426950408889634f;
  const int koff = (32 * w + c16) * 64 + ((g ^ ((-(c16 >> 2)) & 3)) * 16);
  const int voff = 32768 + (32 * w + 4 * g + (c16 >> 2)) * 64 + 8 * (c16 & 3);
  const int vx = (g & 1) * 32;
#pragma unroll
  for (int m = 0; m < 2; m++) {
    f32x4 s[17];
#pragma unroll
    for (int i = 0; i < 17; i++) {
      bf16x8 kf = lds_b128(lds + koff + (i + m) * 1024);
      s[i] = mfma(kf, qf[m], (f32x4){0.f, 0.f, 0.f, 0.f});
    }
    const float ninf = -__builtin_inff();
#pragma unroll
    for (int r = 0; r < 4; r++) {
      if (!(c16 < 4 * g + r)) s[0][r] = ninf;
      if (!(4 * g + r <= c16)) s[16][r] = ninf;
    }
    if (qb == 0) {
      const int nneg = 16 - 2 * w - m;
#pragma unroll
      for (int i = 0; i < 16; i++)
        if (i < nneg) s[i] = (f32x4){ninf, ninf, ninf, ninf};
    }
    float mx = ninf;
#pragma unroll
    for (int i = 0; i < 17; i++) mx = fmaxf(fmaxf(mx, fmaxf(s[i][0], s[i][1])), fmaxf(s[i][2], s[i][3]));
    mx = fmaxf(mx, __shfl_xor(mx, 16));
    mx = fmaxf(mx, __shfl_xor(mx, 32));
    const float nb = -mx * sc;
    unsigned p[18][2];
#pragma unroll
    for (int i = 0; i < 17; i++) {
      float e0 = __builtin_amdgcn_exp2f(__builtin_fmaf(s[i][0], sc, nb));
      float e1 = __builtin_amdgcn_exp2f(__builtin_fmaf(s[i][1], sc, nb));
      float e2 = __builtin_amdgcn_exp2f(__builtin_fmaf(s[i][2], sc, nb));
      float e3 = __builtin_amdgcn_exp2f(__builtin_fmaf(s[i][3], sc, nb));
      p[i][0] = pk2(e0, e1); p[i][1] = pk2(e2, e3);
    }
    p[17][0] = 0; p[17][1] = 0;
    f32x4 o0 = {0.f, 0.f, 0.f, 0.f}, o1 = o0;
#pragma unroll
    for (int i = 0; i < 9; i++) {
      const int r0 = (2 * i + m) * 1024, r1 = (i < 8) ? r0 + 1024 : r0;
      bf16x8 pf = __builtin_bit_cast(bf16x8, (u32x4){p[2 * i][0], p[2 * i][1], p[2 * i + 1][0], p[2 * i + 1][1]});
      bf16x8 va = cat8(lds_tr(lds + voff + vx + r0), lds_tr(lds + voff + vx + r1));
      bf16x8 vb = cat8(lds_tr(lds + voff + (32 ^ vx) + r0), lds_tr(lds + voff + (32 ^ vx) + r1));
      o0 = mfma(va, pf, o0);
      o1 = mfma(vb, pf, o1);
    }
    const float den = __shfl(o1[2], c16);
    const float inv = 1.0f / den;
    char* op = ws + WS_O + (size_t)(q0 + 32 * w + 16 * m + c16) * 1152 + h * 36;
    *(u32x2_a4*)(op + 8 * g) = (u32x2){pk2(o0[0] * inv, o0[1] * inv), pk2(o0[2] * inv, o0[3] * inv)};
    if (g == 0) *(unsigned*)(op + 32) = pk2(o1[0] * inv, o1[1] * inv);
  }
}

constexpr int G_SLOT = 16384 + 18432;
constexpr int G_LDS = 4 * G_SLOT;
template <int NJ> struct Frag2 { bf16x8 a[NJ]; bf16x8 b[4]; };

template <int NJ, int NS, int LDB, int LDA>
DEVI void gemm_core(char* lds, const char* __restrict__ Bb, const char* __restrict__ Ab, int w, f32x4 (&acc)[NJ][4]) {
  const int l = threadIdx.x & 63, g = l >> 4, c16 = l & 15;
  const int fh = w >> 2, th = w & 1, kh = (w >> 1) & 1;
  unsigned bo[2], ao[3];
#pragma unroll
  for (int i = 0; i < 2; i++) {
    int r = (2 * w + i) * 8 + (l >> 3);
    int c = (l & 7) ^ ((r >> 1) & 7);
    bo[i] = r * LDB + c * 16;
  }
#pragma unroll
  for (int i = 0; i < 3; i++) {
    int q = w * 144 + i * 64 + ((i == 2) ? (l & 15) : l);
    int rho = q / 18, c = q - rho * 18;
    ao[i] = swap23(rho) * LDA + c * 16;
  }
  auto issue = [&](int t) {
    char* slot = lds + (t & 3) * G_SLOT;
    const char* bs = Bb + t * 128;
    const char* as = Ab + (size_t)t * (64 * LDA);
    glds16(bs + bo[0], slot + w * 2048);
    glds16(bs + bo[1], slot + w * 2048 + 1024);
    char* ad = slot + 16384 + w * 2304;
    glds16(as + ao[0], ad);
    glds16(as + ao[1], ad + 1024);
    if (l < 16) glds16(as + ao[2], ad + 2048);
  };
  const int boff = (64 * th + c16) * 128 + (((4 * kh + g) ^ (c16 >> 1)) * 16);
  const int aoff = 16384 + (32 * kh + 16 * (g >> 1) + 4 * (g & 1) + (c16 >> 2)) * 288 + fh * 160 + 8 * (c16 & 3);
  auto rd = [&](Frag2<NJ>& f, int t) {
    const char* slot = lds + (t & 3) * G_SLOT;
#pragma unroll
    for (int m = 0; m < 4; m++) f.b[m] = lds_b128(slot + boff + m * 2048);
#pragma unroll
    for (int j = 0; j < NJ; j++)
      f.a[j] = cat8(lds_tr(slot + aoff + j * 32), lds_tr(slot + aoff + j * 32 + 8 * 288));
  };
  auto mm = [&](const Frag2<NJ>& f) {
#pragma unroll
    for (int j = 0; j < NJ; j++)
#pragma unroll
      for (int m = 0; m < 4; m++) acc[j][m] = mfma(f.a[j], f.b[m], acc[j][m]);
  };
#pragma unroll
  for (int j = 0; j < NJ; j++)
#pragma unroll
    for (int m = 0; m < 4; m++) acc[j][m] = (f32x4){0.f, 0.f, 0.f, 0.f};
  Frag2<NJ> f0, f1;
  issue(0); issue(1); issue(2); issue(3);
  wait_vm_lgkm0<15>();
  bar();
  rd(f0, 0);
  constexpr int M = NS - 4, MP = M & ~1;
#pragma unroll 1
  for (int t = 0; t < MP; t += 2) {
    wait_vm_lgkm0<10>(); bar();
    issue(t + 4); rd(f1, t + 1); mm(f0);
    wait_vm_lgkm0<10>(); bar();
    issue(t + 5); rd(f0, t + 2); mm(f1);
  }
  if constexpr (M & 1) {
    wait_vm_lgkm0<10>(); bar(); issue(NS - 1); rd(f1, NS - 4); mm(f0);
    wait_vm_lgkm0<10>(); bar(); rd(f0, NS - 3); mm(f1);
    wait_vm_lgkm0<5>();  bar(); rd(f1, NS - 2); mm(f0);
    wait_vm_lgkm0<0>();  bar(); rd(f0, NS - 1); mm(f1);
    mm(f0);
  } else {
    wait_vm_lgkm0<10>(); bar(); rd(f1, NS - 3); mm(f0);
    wait_vm_lgkm0<5>();  bar(); rd(f0, NS - 2); mm(f1);
    wait_vm_lgkm0<0>();  bar(); rd(f1, NS - 1); mm(f0);
    mm(f1);
  }
}

template <int NJ>
DEVI void khalf_exchange(char* lds, int w, f32x4 (&acc)[NJ][4]) {
  const int l = threadIdx.x & 63, kh = (w >> 1) & 1;
  lds_bar();
  f32x4* mine = (f32x4*)(lds + w * 10240) + l;
  const f32x4* other = (const f32x4*)(lds + (w ^ 2) * 10240) + l;
  if (kh == 0) {
#pragma unroll
    for (int j = 0; j < NJ; j++) { mine[(2 * j) * 64] = acc[j][2]; mine[(2 * j + 1) * 64] = acc[j][3]; }
  } else {
#pragma unroll
    for (int j = 0; j < NJ; j++) {
      mine[(2 * j) * 64] = acc[j][0]; mine[(2 * j + 1) * 64] = acc[j][1];
      acc[j][0] = acc[j][2]; acc[j][1] = acc[j][3];
    }
  }
  lds_bar();
#pragma unroll
  for (int j = 0; j < NJ; j++) { acc[j][0] += other[(2 * j) * 64]; acc[j][1] += other[(2 * j + 1) * 64]; }
}

template <int NJ>
DEVI void k2_body(char* lds, const char* __restrict__ w_sp, const char* __restrict__ b_sp, char* __restrict__ ws,
                  int tb, int fb, int w) {
  f32x4 acc[NJ][4];
  gemm_core<NJ, 32, 4096, 4608>(lds, w_sp + (size_t)tb * 128 * 4096, ws + WS_Z2 + fb * 288, w, acc);
  const int l = threadIdx.x & 63, g = l >> 4, c16 = l & 15;
  const int fh = w >> 2, th = w & 1, kh = (w >> 1) & 1;
  const int trow = tb * 128 + 64 * th + 32 * kh + c16;
  const size_t eo = (size_t)trow * 4608 + (fb * 144 + fh * 80 + 4 * g) * 2;
  u32x2 z[NJ][2];
  float bs[2];
#pragma unroll
  for (int m = 0; m < 2; m++) {
    bs[m] = __builtin_bit_cast(float, (unsigned)(*(const unsigned short*)(b_sp + (trow + 16 * m) * 2)) << 16);
#pragma unroll
    for (int j = 0; j < NJ; j++) z[j][m] = *(const u32x2*)(ws + WS_Z1 + eo + (size_t)m * 16 * 4608 + j * 32);
  }
  khalf_exchange<NJ>(lds, w, acc);
#pragma unroll
  for (int m = 0; m < 2; m++)
#pragma unroll
    for (int j = 0; j < NJ; j++) {
      f32x4 a = acc[j][m];
      u32x2 o = {pk2(bflo(z[j][m][0]) * (a[0] + bs[m]), bfhi(z[j][m][0]) * (a[1] + bs[m])),
                 pk2(bflo(z[j][m][1]) * (a[2] + bs[m]), bfhi(z[j][m][1]) * (a[3] + bs[m]))};
      *(u32x2*)(ws + WS_H + eo + (size_t)m * 16 * 4608 + j * 32) = o;
    }
}
__global__ __launch_bounds__(512, 2) void k2_sgu(const char* __restrict__ w_sp, const char* __restrict__ b_sp,
                                                 char* __restrict__ ws) {
  extern __shared__ __attribute__((aligned(16))) char lds[];
  const int b = blockIdx.x, xcd = b & 7, i = b >> 3;
  const int tb = 8 * (xcd & 1) + (i & 7), fb = 4 * (xcd >> 1) + (i >> 3);
  const int w = __builtin_amdgcn_readfirstlane(threadIdx.x >> 6);
  if (w < 4) k2_body<5>(lds, w_sp, b_sp, ws, tb, fb, w);
  else k2_body<4>(lds, w_sp, b_sp, ws, tb, fb, w);
}

template <int NJ>
DEVI void k3_body(char* lds, const char* __restrict__ wo, const char* __restrict__ w_out, char* __restrict__ ws,
                  int tb, int db, int ks, int w) {
  f32x4 acc[NJ][4];
  if (ks < 3)
    gemm_core<NJ, 12, 4608, 1152>(lds, ws + WS_H + (size_t)tb * 128 * 4608 + ks * 1536,
                                  w_out + (size_t)ks * 768 * 1152 + db * 288, w, acc);
  else
    gemm_core<NJ, 9, 1152, 1152>(lds, ws + WS_O + (size_t)tb * 128 * 1152, wo + db * 288, w, acc);
  khalf_exchange<NJ>(lds, w, acc);
  const int l = threadIdx.x & 63, g = l >> 4, c16 = l & 15;
  const int fh = w >> 2, th = w & 1, kh = (w >> 1) & 1;
  const int trow = tb * 128 + 64 * th + 32 * kh + c16;
  char* dst = ws + WS_PART + ((size_t)ks * S_ + trow) * 2304 + (db * 144 + fh * 80 + 4 * g) * 4;
#pragma unroll
  for (int m = 0; m < 2; m++)
#pragma unroll
    for (int j = 0; j < NJ; j++) *(f32x4*)(dst + (size_t)m * 16 * 2304 + j * 64) = acc[j][m];
}
__global__ __launch_bounds__(512, 2) void k3_out(const char* __restrict__ wo, const char* __restrict__ w_out,
                                                 char* __restrict__ ws) {
  extern __shared__ __attribute__((aligned(16))) char lds[];
  const int b = blockIdx.x, xcd = b & 7, i = b >> 3;
  const int ks = xcd >> 1, tb = 8 * (xcd & 1) + (i & 7), db = i >> 3;
  const int w = __builtin_amdgcn_readfirstlane(threadIdx.x >> 6);
  if (w < 4) k3_body<5>(lds, wo, w_out, ws, tb, db, ks, w);
  else k3_body<4>(lds, wo, w_out, ws, tb, db, ks, w);
}

__global__ __launch_bounds__(256) void k4_fin(const char* __restrict__ x, const char* __restrict__ ws,
                                              char* __restrict__ out) {
  const size_t i = (size_t)blockIdx.x * 256 + threadIdx.x;
  const f32x4* p = (const f32x4*)(ws + WS_PART);
  const size_t N = (size_t)S_ * D_ / 4;
  f32x4 bsum = p[i] + p[N + i] + p[2 * N + i], s = p[3 * N + i];
  u32x2 xv = *(const u32x2*)(x + i * 8);
  float xf[4] = {bflo(xv[0]), bfhi(xv[0]), bflo(xv[1]), bfhi(xv[1])};
  float o[4];
#pragma unroll
  for (int e = 0; e < 4; e++) {
    float y = bsum[e];
    float sig = __builtin_amdgcn_rcpf(1.0f + __builtin_amdgcn_exp2f(-1.4426950408889634f * y));
    o[e] = xf[e] + s[e] * (y * sig);
  }
  *(u32x2*)(out + i * 8) = (u32x2){pk2(o[0], o[1]), pk2(o[2], o[3])};
}

extern "C" void kernel_launch(void* const* d_in, const int* in_sizes, int n_in, void* d_out, int out_size,
                              void* d_ws, size_t ws_size, hipStream_t stream) {
  const char* x = (const char*)d_in[0];
  const char* wqkv = (const char*)d_in[1];
  const char* wo = (const char*)d_in[2];
  const char* w_in = (const char*)d_in[4];
  const char* w_sp = (const char*)d_in[5];
  const char* b_sp = (const char*)d_in[6];
  const char* w_out = (const char*)d_in[7];
  char* ws = (char*)d_ws;
  (void)hipFuncSetAttribute((const void*)k1_proj, hipFuncAttributeMaxDynamicSharedMemorySize, K1_LDS);
  (void)hipFuncSetAttribute((const void*)k_attn, hipFuncAttributeMaxDynamicSharedMemorySize, AT_LDS);
  (void)hipFuncSetAttribute((const void*)k2_sgu, hipFuncAttributeMaxDynamicSharedMemorySize, G_LDS);
  (void)hipFuncSetAttribute((const void*)k3_out, hipFuncAttributeMaxDynamicSharedMemorySize, G_LDS);
  k0_ln<<<256, 64, 0, stream>>>(x, ws);
  k1_proj<<<256, 512, K1_LDS, stream>>>(w_in, wqkv, ws);
  k_attn<<<256, 512, AT_LDS, stream>>>(ws);
  k2_sgu<<<256, 512, G_LDS, stream>>>(w_sp, b_sp, ws);
  k3_out<<<256, 512, G_LDS, stream>>>(wo, w_out, ws);
  k4_fin<<<S_ * D_ / 4 / 256, 256, 0, stream>>>(x, ws, (char*)d_out);
}
```

```cpp
#include <hip/hip_runtime.h>
#include <hip/hip_bf16.h>
#include <stdint.h>
#pragma clang diagnostic ignored "-Winline-asm"

typedef __attribute__((ext_vector_type(8))) __bf16 bf16x8;
typedef __attribute__((ext_vector_type(4))) __bf16 bf16x4;
typedef __attribute__((ext_vector_type(2))) __bf16 bf16x2;
typedef __attribute__((ext_vector_type(4))) float f32x4;
typedef __attribute__((ext_vector_type(2))) float f32x2;
typedef __attribute__((ext_vector_type(4))) unsigned u32x4;
typedef __attribute__((ext_vector_type(2))) unsigned u32x2;
typedef u32x4 u32x4_a4 __attribute__((aligned(4)));
typedef u32x2 u32x2_a4 __attribute__((aligned(4)));
#define AS1 __attribute__((address_space(1)))
#define AS3 __attribute__((address_space(3)))
#define DEVI __device__ __forceinline__

constexpr int S_ = 2048, D_ = 576, FH = 2304;

constexpr size_t WS_XN   = 0;
constexpr size_t WS_Z1   = WS_XN + (size_t)S_ * D_ * 2;
constexpr size_t WS_Z2   = WS_Z1 + (size_t)S_ * FH * 2;
constexpr size_t WS_H    = WS_Z2 + (size_t)S_ * FH * 2;
constexpr size_t WS_QKV  = WS_H + (size_t)S_ * FH * 2;
constexpr size_t WS_O    = WS_QKV + (size_t)96 * S_ * 18 * 2;
constexpr size_t WS_PART = WS_O + (size_t)S_ * D_ * 2;
constexpr size_t WS_STAT = WS_PART + (size_t)4 * S_ * D_ * 4;
constexpr size_t WS_WF   = WS_STAT + (size_t)16 * 16 * 128 * 8;

DEVI void glds16(const char* g, char* lds) {
  __builtin_amdgcn_global_load_lds((const AS1 void*)g, (AS3 void*)lds, 16, 0, 0);
}
DEVI void glds_s(unsigned voff, const char* sbase, unsigned ldsa) {
  asm volatile("s_mov_b32 m0, %2\n\ts_nop 0\n\tglobal_load_lds_dwordx4 %0, %1" ::"v"(voff), "s"(sbase), "s"(ldsa) : "memory", "m0");
}
DEVI void glds_s16(unsigned voff, const char* sbase, unsigned ldsa) {
  asm volatile("s_mov_b32 m0, %2\n\ts_mov_b64 exec, 0xffff\n\tglobal_load_lds_dwordx4 %0, %1\n\ts_mov_b64 exec, -1"
               ::"v"(voff), "s"(sbase), "s"(ldsa) : "memory", "m0");
}
#define SB() __builtin_amdgcn_sched_barrier(0)
#define TS(K, I)
#define TW(K, I)
DEVI bf16x8 lds_b128(const char* p) { return *(const AS3 bf16x8*)p; }
DEVI bf16x4 lds_tr(const char* p) { return __builtin_amdgcn_ds_read_tr16_b64_v4bf16((AS3 bf16x4*)p); }
DEVI bf16x8 cat8(bf16x4 a, bf16x4 b) { return __builtin_shufflevector(a, b, 0, 1, 2, 3, 4, 5, 6, 7); }
DEVI f32x4 mfma(bf16x8 a, bf16x8 b, f32x4 c) { return __builtin_amdgcn_mfma_f32_16x16x32_bf16(a, b, c, 0, 0, 0); }
DEVI unsigned pk2(float a, float b) {
  f32x2 v = {a, b};
  bf16x2 r = __builtin_convertvector(v, bf16x2);
  return __builtin_bit_cast(unsigned, r);
}
DEVI float bflo(unsigned u) { return __builtin_bit_cast(float, u << 16); }
DEVI float bfhi(unsigned u) { return __builtin_bit_cast(float, u & 0xffff0000u); }
template <int N> DEVI void wait_vm_lgkm0() { asm volatile("s_waitcnt vmcnt(%0) lgkmcnt(0)" ::"n"(N) : "memory"); }
DEVI void bar() { __builtin_amdgcn_s_barrier(); asm volatile("" ::: "memory"); }
DEVI void lds_bar() { asm volatile("s_waitcnt lgkmcnt(0)" ::: "memory"); bar(); }
DEVI float gelu_t(float y) {
  float p = __builtin_fmaf(y * y, -0.1029432397f, -2.3022081983f);
  float e = __builtin_amdgcn_exp2f(y * p);
  return y * __builtin_amdgcn_rcpf(1.0f + e);
}
DEVI int swap23(int r) { return (r & ~12) | ((r & 4) << 1) | ((r & 8) >> 1); }

DEVI void wprep(const char* __restrict__ wo, const char* __restrict__ w_out, char* __restrict__ ws, int kb, int dq, int l) {
  const int g = l >> 4, c16 = l & 15;
  unsigned short v[4][8];
#pragma unroll
  for (int e = 0; e < 8; e++) {
    const int r = e & 3, J = 2 * (kb < 72 ? kb : kb - 72) + (e >> 2);
    const char* src;
    if (kb < 72) src = w_out + (size_t)(16 * J + 4 * g + r) * 1152;
    else if (J < 32) src = wo + (size_t)(18 * J + 4 * g + r) * 1152;
    else src = wo + (size_t)((8 * (J - 32) + 2 * g + (r >> 1)) * 18 + 16 + (r & 1)) * 1152;
#pragma unroll
    for (int j = 0; j < 4; j++) v[j][e] = *(const unsigned short*)(src + (dq * 64 + j * 16 + c16) * 2);
  }
#pragma unroll
  for (int j = 0; j < 4; j++) {
    u32x4 o;
#pragma unroll
    for (int e = 0; e < 4; e++) o[e] = (unsigned)v[j][2 * e] | ((unsigned)v[j][2 * e + 1] << 16);
    *(u32x4*)(ws + WS_WF + ((size_t)kb * 36 + dq * 4 + j) * 1024 + l * 16) = o;
  }
}

__global__ __launch_bounds__(64) void k0_ln(const char* __restrict__ x, const char* __restrict__ wo,
                                            const char* __restrict__ w_out, char* __restrict__ ws) {
  const int l = threadIdx.x, b = blockIdx.x;
  if (b >= 256) { wprep(wo, w_out, ws, (b - 256) / 9, (b - 256) % 9, l); return; }
  const int row = b * 8 + (l >> 3), c0 = l & 7;
  char* ws_dbg = ws; TS(0, 0);
  const char* src = x + (size_t)row * 1152 + c0 * 16;
  u32x4 v[9];
#pragma unroll
  for (int i = 0; i < 9; i++) v[i] = *(const u32x4*)(src + i * 128);
  *(u32x4*)(ws + WS_STAT + (size_t)b * 1024 + l * 16) = (u32x4){~0u, ~0u, ~0u, ~0u};
  float s = 0.f;
#pragma unroll
  for (int i = 0; i < 9; i++)
#pragma unroll
    for (int e = 0; e < 4; e++) s += bflo(v[i][e]) + bfhi(v[i][e]);
  s += __shfl_xor(s, 1); s += __shfl_xor(s, 2); s += __shfl_xor(s, 4);
  const float mean = s * (1.0f / 576.0f);
  float q = 0.f;
#pragma unroll
  for (int i = 0; i < 9; i++)
#pragma unroll
    for (int e = 0; e < 4; e++) {
      float a = bflo(v[i][e]) - mean, c = bfhi(v[i][e]) - mean;
      q += a * a + c * c;
    }
  q += __shfl_xor(q, 1); q += __shfl_xor(q, 2); q += __shfl_xor(q, 4);
  const float rs = 1.0f / __builtin_sqrtf(q * (1.0f / 576.0f) + 1e-5f);
  char* dst = ws + WS_XN + (size_t)row * 1152 + c0 * 16;
#pragma unroll
  for (int i = 0; i < 9; i++) {
    u32x4 o;
#pragma unroll
    for (int e = 0; e < 4; e++) o[e] = pk2((bflo(v[i][e]) - mean) * rs, (bfhi(v[i][e]) - mean) * rs);
    *(u32x4*)(dst + i * 128) = o;
  }
  TS(0, 4);
}

constexpr int K1_SLOT = 8192 + 25600;
constexpr int K1_STAT = 4 * K1_SLOT;
constexpr int K1_STAT2 = K1_STAT + 4096;
constexpr int K1_LDS = K1_STAT2 + 4096;

template <int NJ> struct Frag1 { bf16x8 a[NJ]; bf16x8 b[4]; };

DEVI void k1_sub(int J, int& ty, int& ix) {
  if (J < 7)       { if (J < 3) { ty = 0; ix = J; } else if (J < 5) { ty = 1; ix = J - 3; } else { ty = 2; ix = J - 5; } }
  else if (J < 13) { int j = J - 7;  if (j < 2) { ty = 0; ix = 3 + j; } else if (j < 5) { ty = 1; ix = j; } else { ty = 2; ix = 2; } }
  else if (J < 19) { int j = J - 13; if (j < 2) { ty = 0; ix = 5 + j; } else if (j < 4) { ty = 1; ix = 3 + j; } else { ty = 2; ix = j - 1; } }
  else             { int j = J - 19; if (j < 2) { ty = 0; ix = 7 + j; } else if (j < 4) { ty = 1; ix = 5 + j; } else { ty = 2; ix = j + 1; } }
}

template <int NQ>
DEVI void k1_body(char* lds, const char* __restrict__ w_in, const char* __restrict__ wqkv, char* __restrict__ ws,
                  int rb, int cg, int w, int sh) {
  constexpr int NJ = (NQ == 0) ? 7 : 6;
  constexpr int J0 = (NQ == 0) ? 0 : (NQ == 1) ? 7 : (NQ == 2) ? 13 : 19;
  constexpr int NZ2 = (NQ == 0) ? 3 : 2;
  constexpr int NZ1 = (NQ == 1) ? 3 : 2;
  constexpr int Z2I = (NQ == 0) ? 0 : (NQ == 1) ? 3 : (NQ == 2) ? 5 : 7;
  constexpr int Z1I = (NQ == 0) ? 0 : (NQ == 1) ? 2 : (NQ == 2) ? 5 : 7;
  constexpr int QI  = (NQ == 0) ? 0 : (NQ == 1) ? 2 : (NQ == 2) ? 3 : 5;
  constexpr int NS = 18;
  char* ws_dbg = ws; TS(1, 0);
  const int tid = threadIdx.x, l = tid & 63, g = l >> 4, c16 = l & 15;
  const int qstart = 108 * cg - ((cg & 1) ? 4 : 0);

  const char* xsrc;
  {
    int r = 16 * w + (l >> 2);
    int c = (l & 3) ^ ((-(r >> 2)) & 3);
    xsrc = ws + WS_XN + (size_t)(rb * 128 + r) * 1152 + c * 16;
  }
  const char* wsrc[4];
  unsigned winc[4];
#pragma unroll
  for (int i = 0; i < 4; i++) {
    int q = w * 200 + i * 64 + l;
    if (i == 3) q = w * 200 + 192 + (l & 7);
    int rho = q / 50, c = q - rho * 50;
    int k = swap23(rho);
    int ty, ix;
    k1_sub(c >> 1, ty, ix);
    int col = ix * 16 + (c & 1) * 8;
    if (ty == 2) { wsrc[i] = wqkv + (size_t)k * 3456 + (qstart + col) * 2; winc[i] = 32 * 3456; }
    else { wsrc[i] = w_in + (size_t)k * 9216 + ((ty == 0 ? FH : 0) + 144 * cg + col) * 2; winc[i] = 32 * 9216; }
  }
  auto issue = [&](int t) {
    char* slot = lds + (t & 3) * K1_SLOT;
    glds16(xsrc + t * 64, slot + w * 1024);
    char* wd = slot + 8192 + w * 3200;
#pragma unroll
    for (int i = 0; i < 3; i++) glds16(wsrc[i] + (size_t)winc[i] * (unsigned)t, wd + i * 1024);
    if (l < 8) glds16(wsrc[3] + (size_t)winc[3] * (unsigned)t, wd + 3072);
  };
  const int boff = (64 * sh + c16) * 64 + ((g ^ ((-(c16 >> 2)) & 3)) * 16);
  const int aoff = 8192 + (16 * (g >> 1) + 4 * (g & 1) + (c16 >> 2)) * 800 + J0 * 32 + 8 * (c16 & 3);
  auto rd = [&](Frag1<NJ>& f, int t) {
    const char* slot = lds + (t & 3) * K1_SLOT;
#pragma unroll
    for (int m = 0; m < 4; m++) f.b[m] = lds_b128(slot + boff + m * 1024);
#pragma unroll
    for (int j = 0; j < NJ; j++)
      f.a[j] = cat8(lds_tr(slot + aoff + j * 32), lds_tr(slot + aoff + j * 32 + 8 * 800));
  };
  f32x4 acc[NJ][4];
#pragma unroll
  for (int j = 0; j < NJ; j++)
#pragma unroll
    for (int m = 0; m < 4; m++) acc[j][m] = (f32x4){0.f, 0.f, 0.f, 0.f};
  auto mm = [&](const Frag1<NJ>& f) {
#pragma unroll
    for (int j = 0; j < NJ; j++)
#pragma unroll
      for (int m = 0; m < 4; m++) acc[j][m] = mfma(f.a[j], f.b[m], acc[j][m]);
  };
  Frag1<NJ> f0, f1;
  issue(0); issue(1); issue(2); issue(3);
  wait_vm_lgkm0<15>();
  bar();
  TS(1, 1);
  rd(f0, 0);
#pragma unroll 1
  for (int t = 0; t < NS - 4; t += 2) {
    wait_vm_lgkm0<10>(); bar();
    issue(t + 4); rd(f1, t + 1); mm(f0);
    wait_vm_lgkm0<10>(); bar();
    issue(t + 5); rd(f0, t + 2); mm(f1);
  }
  wait_vm_lgkm0<10>(); bar(); rd(f1, NS - 3); mm(f0);
  wait_vm_lgkm0<5>();  bar(); rd(f0, NS - 2); mm(f1);
  wait_vm_lgkm0<0>();  bar(); rd(f1, NS - 1); mm(f0);
  mm(f1);
  TS(1, 2);
  const int srow = rb * 128 + 64 * sh + c16;
  {
    float s1[4], s2[4];
#pragma unroll
    for (int m = 0; m < 4; m++) {
      float a = 0.f, q = 0.f;
#pragma unroll
      for (int j = 0; j < NZ2; j++)
#pragma unroll
        for (int r = 0; r < 4; r++) {
          float z = gelu_t(acc[j][m][r]);
          acc[j][m][r] = z;
          a += z; q = __builtin_fmaf(z, z, q);
        }
      a += __shfl_xor(a, 16); q += __shfl_xor(q, 16);
      a += __shfl_xor(a, 32); q += __shfl_xor(q, 32);
      s1[m] = a; s2[m] = q;
    }
    f32x2* st = (f32x2*)(lds + K1_STAT) + NQ * 128 + 64 * sh + c16;
    if (g == 0) {
#pragma unroll
      for (int m = 0; m < 4; m++) st[16 * m] = (f32x2){s1[m], s2[m]};
    }
  }
  lds_bar();
  AS1 unsigned long long* gstat = (AS1 unsigned long long*)(ws + WS_STAT) + (size_t)rb * 16 * 128;
  if (tid < 128) {
    const f32x2* st = (const f32x2*)(lds + K1_STAT) + tid;
    f32x2 v = st[0] + st[128] + st[256] + st[384];
    const float v0 = v[0], v1 = v[1];
    unsigned lo = __builtin_bit_cast(unsigned, v0), hi = __builtin_bit_cast(unsigned, v1);
    if (hi == 0xffffffffu) hi = 0x7fc00000u;
    __hip_atomic_store(gstat + cg * 128 + tid, ((unsigned long long)hi << 32) | lo, __ATOMIC_RELAXED,
                       __HIP_MEMORY_SCOPE_AGENT);
  }
#pragma unroll
  for (int j = 0; j < NZ1; j++)
#pragma unroll
    for (int m = 0; m < 4; m++) {
      f32x4 v = acc[NZ2 + j][m];
      u32x2 o = {pk2(gelu_t(v[0]), gelu_t(v[1])), pk2(gelu_t(v[2]), gelu_t(v[3]))};
      *(u32x2*)(ws + WS_Z1 + (size_t)(srow + 16 * m) * 4608 + (144 * cg + (Z1I + j) * 16 + 4 * g) * 2) = o;
    }
#pragma unroll
  for (int j = NZ2 + NZ1; j < NJ; j++) {
    const int cl = (QI + (j - NZ2 - NZ1)) * 16 + 4 * g + qstart;
#pragma unroll
    for (int p = 0; p < 2; p++) {
      const int c = cl + 2 * p;
      const bool ok = (c >= 108 * cg) && (c < 108 * cg + 108);
      const int hp = c / 18, d = c - hp * 18;
      char* dst = ws + WS_QKV + ((size_t)hp * S_ + srow) * 36 + d * 2;
      if (ok) {
#pragma unroll
        for (int m = 0; m < 4; m++) *(unsigned*)(dst + m * 16 * 36) = pk2(acc[j][m][2 * p], acc[j][m][2 * p + 1]);
      }
    }
  }
  TS(1, 3);
  {
    const int row = tid & 127, part = tid >> 7;
    float a = 0.f, q = 0.f;
#pragma unroll
    for (int i = 0; i < 4; i++) {
      unsigned long long v;
      do {
        v = __hip_atomic_load(gstat + (part * 4 + i) * 128 + row, __ATOMIC_RELAXED, __HIP_MEMORY_SCOPE_AGENT);
      } while ((unsigned)(v >> 32) == 0xffffffffu);
      a += __builtin_bit_cast(float, (unsigned)v);
      q += __builtin_bit_cast(float, (unsigned)(v >> 32));
    }
    ((f32x2*)(lds + K1_STAT2))[part * 128 + row] = (f32x2){a, q};
  }
  lds_bar();
#pragma unroll
  for (int m = 0; m < 4; m++) {
    const f32x2* st = (const f32x2*)(lds + K1_STAT2) + 64 * sh + 16 * m + c16;
    f32x2 v = st[0] + st[128] + st[256] + st[384];
    const float mean = v[0] * (1.0f / FH);
    const float var = fmaxf(v[1] * (1.0f / FH) - mean * mean, 0.f);
    const float rs = 1.0f / __builtin_sqrtf(var + 1e-5f);
#pragma unroll
    for (int j = 0; j < NZ2; j++) {
      f32x4 z = acc[j][m];
      u32x2 o = {pk2((z[0] - mean) * rs, (z[1] - mean) * rs), pk2((z[2] - mean) * rs, (z[3] - mean) * rs)};
      *(u32x2*)(ws + WS_Z2 + (size_t)(srow + 16 * m) * 4608 + (144 * cg + (Z2I + j) * 16 + 4 * g) * 2) = o;
    }
  }
  TS(1, 4);
}

__global__ __launch_bounds__(512, 2) void k1_proj(const char* __restrict__ w_in, const char* __restrict__ wqkv,
                                                  char* __restrict__ ws) {
  extern __shared__ __attribute__((aligned(16))) char lds[];
  const int b = blockIdx.x, xcd = b & 7, i = b >> 3;
  const int rb = 8 * (xcd & 1) + (i & 7), cg = 4 * (xcd >> 1) + (i >> 3);
  const int w = __builtin_amdgcn_readfirstlane(threadIdx.x >> 6);
  const int sh = w >> 2, nq = sh ? 3 - (w & 3) : (w & 3);
  if (nq == 0) k1_body<0>(lds, w_in, wqkv, ws, rb, cg, w, sh);
  else if (nq == 1) k1_body<1>(lds, w_in, wqkv, ws, rb, cg, w, sh);
  else if (nq == 2) k1_body<2>(lds, w_in, wqkv, ws, rb, cg, w, sh);
  else k1_body<3>(lds, w_in, wqkv, ws, rb, cg, w, sh);
}

constexpr int AT_LDS = 65536;
__global__ __launch_bounds__(512, 2) void k_attn(char* __restrict__ ws) {
  extern __shared__ __attribute__((aligned(16))) char lds[];
  const int b = blockIdx.x, h = b & 31, qb = b >> 5;
  const int tid = threadIdx.x, l = tid & 63, g = l >> 4, c16 = l & 15;
  const int w = __builtin_amdgcn_readfirstlane(tid >> 6);
  const int q0 = qb * 256;
  char* ws_dbg = ws; TS(4, 0);
  const char* qkv = ws + WS_QKV;
  {
    const int key = q0 - 256 + tid;
    u32x4 k0 = {0, 0, 0, 0}, k1 = k0, v0 = k0, v1 = k0;
    unsigned k2 = 0, v2 = 0;
    if (key >= 0) {
      const char* kp = qkv + ((size_t)(32 + h) * S_ + key) * 36;
      const char* vp = qkv + ((size_t)(64 + h) * S_ + key) * 36;
      k0 = *(const u32x4_a4*)kp; k1 = *(const u32x4_a4*)(kp + 16); k2 = *(const unsigned*)(kp + 32);
      v0 = *(const u32x4_a4*)vp; v1 = *(const u32x4_a4*)(vp + 16); v2 = *(const unsigned*)(vp + 32);
    }
    const int f = (-(tid >> 2)) & 3;
    char* kr = lds + tid * 64;
    *(u32x4*)(kr + ((0 ^ f) * 16)) = k0;
    *(u32x4*)(kr + ((1 ^ f) * 16)) = k1;
    *(u32x4*)(kr + ((2 ^ f) * 16)) = (u32x4){k2, 0, 0, 0};
    *(u32x4*)(kr + ((3 ^ f) * 16)) = (u32x4){0, 0, 0, 0};
    char* vr = lds + 32768 + tid * 64;
    const int x = ((tid >> 2) & 1) * 32;
    *(u32x4*)(vr + (0 ^ x)) = v0;
    *(u32x4*)(vr + (16 ^ x)) = v1;
    *(u32x4*)(vr + (32 ^ x)) = (u32x4){v2, 0x00003f80u, 0, 0};
    *(u32x4*)(vr + (48 ^ x)) = (u32x4){0, 0, 0, 0};
  }
  bf16x8 qf[2];
#pragma unroll
  for (int m = 0; m < 2; m++) {
    const char* qp = qkv + ((size_t)h * S_ + q0 + 32 * w + 16 * m + c16) * 36;
    u32x4 v = {0, 0, 0, 0};
    if (g < 2) v = *(const u32x4_a4*)(qp + 16 * g);
    else if (g == 2) v[0] = *(const unsigned*)(qp + 32);
    qf[m] = __builtin_bit_cast(bf16x8, v);
  }
  __syncthreads();
  const float sc = 0.23570226039551584f * 1.4426950408889634f;
  const int koff = (32 * w + c16) * 64 + ((g ^ ((-(c16 >> 2)) & 3)) * 16);
  const int voff = 32768 + (32 * w + 4 * g + (c16 >> 2)) * 64 + 8 * (c16 & 3);
  const int vx = (g & 1) * 32;
#pragma unroll
  for (int m = 0; m < 2; m++) {
    f32x4 s[17];
#pragma unroll
    for (int i = 0; i < 17; i++) {
      bf16x8 kf = lds_b128(lds + koff + (i + m) * 1024);
      s[i] = mfma(kf, qf[m], (f32x4){0.f, 0.f, 0.f, 0.f});
    }
    const float ninf = -__builtin_inff();
#pragma unroll
    for (int r = 0; r < 4; r++) {
      if (!(c16 < 4 * g + r)) s[0][r] = ninf;
      if (!(4 * g + r <= c16)) s[16][r] = ninf;
    }
    if (qb == 0) {
      const int nneg = 16 - 2 * w - m;
#pragma unroll
      for (int i = 0; i < 16; i++)
        if (i < nneg) s[i] = (f32x4){ninf, ninf, ninf, ninf};
    }
    float mx = ninf;
#pragma unroll
    for (int i = 0; i < 17; i++) mx = fmaxf(fmaxf(mx, fmaxf(s[i][0], s[i][1])), fmaxf(s[i][2], s[i][3]));
    mx = fmaxf(mx, __shfl_xor(mx, 16));
    mx = fmaxf(mx, __shfl_xor(mx, 32));
    const float nb = -mx * sc;
    unsigned p[18][2];
#pragma unroll
    for (int i = 0; i < 17; i++) {
      float e0 = __builtin_amdgcn_exp2f(__builtin_fmaf(s[i][0], sc, nb));
      float e1 = __builtin_amdgcn_exp2f(__builtin_fmaf(s[i][1], sc, nb));
      float e2 = __builtin_amdgcn_exp2f(__builtin_fmaf(s[i][2], sc, nb));
      float e3 = __builtin_amdgcn_exp2f(__builtin_fmaf(s[i][3], sc, nb));
      p[i][0] = pk2(e0, e1); p[i][1] = pk2(e2, e3);
    }
    p[17][0] = 0; p[17][1] = 0;
    f32x4 o0 = {0.f, 0.f, 0.f, 0.f}, o1 = o0;
#pragma unroll
    for (int i = 0; i < 9; i++) {
      const int r0 = (2 * i + m) * 1024, r1 = (i < 8) ? r0 + 1024 : r0;
      bf16x8 pf = __builtin_bit_cast(bf16x8, (u32x4){p[2 * i][0], p[2 * i][1], p[2 * i + 1][0], p[2 * i + 1][1]});
      bf16x8 va = cat8(lds_tr(lds + voff + vx + r0), lds_tr(lds + voff + vx + r1));
      bf16x8 vb = cat8(lds_tr(lds + voff + (32 ^ vx) + r0), lds_tr(lds + voff + (32 ^ vx) + r1));
      o0 = mfma(va, pf, o0);
      o1 = mfma(vb, pf, o1);
    }
    const float den = __shfl(o1[2], c16);
    const float inv = 1.0f / den;
    char* op = ws + WS_O + (size_t)(16 * qb + 2 * w + m) * 18432;
    *(u32x2*)(op + (h >> 1) * 1024 + l * 16 + (h & 1) * 8) = (u32x2){pk2(o0[0] * inv, o0[1] * inv), pk2(o0[2] * inv, o0[3] * inv)};
    if (g == 0)
      *(unsigned*)(op + (16 + (h >> 4)) * 1024 + (((h & 7) >> 1) * 16 + c16) * 16 + ((h >> 3) & 1) * 8 + (h & 1) * 4) =
          pk2(o1[0] * inv, o1[1] * inv);
  }
  TS(4, 4);
}

constexpr int G_SLOT = 16384 + 18432;
constexpr int G_LDS = 4 * G_SLOT;
template <int NJ> struct Frag2 { bf16x8 a[NJ]; bf16x8 b[4]; };

template <int NJ, int NS, int LDB, int LDA>
DEVI void gemm_core(char* lds, const char* __restrict__ Bb, const char* __restrict__ Ab, int w, f32x4 (&acc)[NJ][4], char* ws_dbg, int kid) {
  TS(kid, 0);
  const int l = threadIdx.x & 63, g = l >> 4, c16 = l & 15;
  const int fh = w >> 2, th = w & 1, kh = (w >> 1) & 1;
  unsigned bo[2], ao[3];
#pragma unroll
  for (int i = 0; i < 2; i++) {
    int r = (2 * w + i) * 8 + (l >> 3);
    int c = (l & 7) ^ ((r >> 1) & 7);
    bo[i] = r * LDB + c * 16;
  }
#pragma unroll
  for (int i = 0; i < 3; i++) {
    int q = w * 144 + i * 64 + ((i == 2) ? (l & 15) : l);
    int rho = q / 18, c = q - rho * 18;
    ao[i] = swap23(rho) * LDA + c * 16;
  }
  const unsigned lb = (unsigned)(uintptr_t)(AS3 char*)lds;
  const unsigned bdst = lb + w * 2048, adst = lb + 16384 + w * 2304;
  auto piece = [&](int t, int i) {
    const unsigned so = (t & 3) * G_SLOT;
    const size_t astep = 64 * LDA;
    if (i < 2) glds_s(bo[i], Bb + t * 128, bdst + so + i * 1024);
    else if (i < 4) glds_s(ao[i - 2], Ab + (size_t)t * astep, adst + so + (i - 2) * 1024);
    else glds_s16(ao[2], Ab + (size_t)t * astep, adst + so + 2048);
  };
  auto issue = [&](int t) {
#pragma unroll
    for (int i = 0; i < 5; i++) piece(t, i);
  };
  const int boff = (64 * th + c16) * 128 + (((4 * kh + g) ^ (c16 >> 1)) * 16);
  const int aoff = 16384 + (32 * kh + 16 * (g >> 1) + 4 * (g & 1) + (c16 >> 2)) * 288 + fh * 160 + 8 * (c16 & 3);
  auto rd = [&](Frag2<NJ>& f, int t) {
    const char* slot = lds + (t & 3) * G_SLOT;
#pragma unroll
    for (int m = 0; m < 4; m++) f.b[m] = lds_b128(slot + boff + m * 2048);
#pragma unroll
    for (int j = 0; j < NJ; j++)
      f.a[j] = cat8(lds_tr(slot + aoff + j * 32), lds_tr(slot + aoff + j * 32 + 8 * 288));
  };
  auto step = [&](const Frag2<NJ>& cur, Frag2<NJ>& nxt, int t, bool doissue) {
    const char* slot = lds + ((t + 1) & 3) * G_SLOT;
    const char* pb = slot + boff;
    const char* pa = slot + aoff;
    bf16x4 lo[NJ];
#pragma unroll
    for (int x = 0; x < 4 * NJ; x++) {
      const int j = x >> 2, m = x & 3;
      acc[j][m] = mfma(cur.a[j], cur.b[m], acc[j][m]);
      if (x < 4) nxt.b[x] = lds_b128(pb + x * 2048);
      else if (x < 4 + 2 * NJ) {
        const int y = x - 4, jj = y >> 1;
        if ((y & 1) == 0) lo[jj] = lds_tr(pa + jj * 32);
        else nxt.a[jj] = cat8(lo[jj], lds_tr(pa + jj * 32 + 8 * 288));
      }
      if (doissue && (x % 3) == 1 && x / 3 < 5) piece(t + 4, x / 3);
      SB();
    }
  };
  auto mm = [&](const Frag2<NJ>& f) {
#pragma unroll
    for (int j = 0; j < NJ; j++)
#pragma unroll
      for (int m = 0; m < 4; m++) acc[j][m] = mfma(f.a[j], f.b[m], acc[j][m]);
  };
#pragma unroll
  for (int j = 0; j < NJ; j++)
#pragma unroll
    for (int m = 0; m < 4; m++) acc[j][m] = (f32x4){0.f, 0.f, 0.f, 0.f};
  Frag2<NJ> f0, f1;
  issue(0); issue(1); issue(2); issue(3);
  wait_vm_lgkm0<15>();
  bar();
  TS(kid, 1);
  rd(f0, 0);
  constexpr int M = NS - 4, MP = M & ~1;
#pragma unroll 1
  for (int t = 0; t < MP; t += 2) {
    wait_vm_lgkm0<10>(); bar(); SB();
    step(f0, f1, t, true);
    wait_vm_lgkm0<10>(); bar(); SB();
    step(f1, f0, t + 1, true);
  }
  if constexpr (M & 1) {
    wait_vm_lgkm0<10>(); bar(); SB(); step(f0, f1, NS - 5, true);
    wait_vm_lgkm0<10>(); bar(); SB(); step(f1, f0, NS - 4, false);
    wait_vm_lgkm0<5>();  bar(); SB(); step(f0, f1, NS - 3, false);
    wait_vm_lgkm0<0>();  bar(); SB(); step(f1, f0, NS - 2, false);
    mm(f0);
  } else {
    wait_vm_lgkm0<10>(); bar(); SB(); step(f0, f1, NS - 4, false);
    wait_vm_lgkm0<5>();  bar(); SB(); step(f1, f0, NS - 3, false);
    wait_vm_lgkm0<0>();  bar(); SB(); step(f0, f1, NS - 2, false);
    mm(f1);
  }
  TS(kid, 2);
}

template <int NJ>
DEVI void khalf_exchange(char* lds, int w, f32x4 (&acc)[NJ][4]) {
  const int l = threadIdx.x & 63, kh = (w >> 1) & 1;
  lds_bar();
  f32x4* mine = (f32x4*)(lds + w * 10240) + l;
  const f32x4* other = (const f32x4*)(lds + (w ^ 2) * 10240) + l;
  if (kh == 0) {
#pragma unroll
    for (int j = 0; j < NJ; j++) { mine[(2 * j) * 64] = acc[j][2]; mine[(2 * j + 1) * 64] = acc[j][3]; }
  } else {
#pragma unroll
    for (int j = 0; j < NJ; j++) {
      mine[(2 * j) * 64] = acc[j][0]; mine[(2 * j + 1) * 64] = acc[j][1];
      acc[j][0] = acc[j][2]; acc[j][1] = acc[j][3];
    }
  }
  lds_bar();
#pragma unroll
  for (int j = 0; j < NJ; j++) { acc[j][0] += other[(2 * j) * 64]; acc[j][1] += other[(2 * j + 1) * 64]; }
}

template <int NJ>
DEVI void k2_body(char* lds, const char* __restrict__ w_sp, const char* __restrict__ b_sp, char* __restrict__ ws,
                  int tb, int fb, int w) {
  f32x4 acc[NJ][4];
  gemm_core<NJ, 32, 4096, 4608>(lds, w_sp + (size_t)tb * 128 * 4096,
     ws + WS_Z2 + fb * 288,
     w, acc, ws, 2);
  const int l = threadIdx.x & 63, g = l >> 4, c16 = l & 15;
  const int fh = w >> 2, th = w & 1, kh = (w >> 1) & 1;
  const int trow = tb * 128 + 64 * th + 32 * kh + c16;
  const size_t eo = (size_t)trow * 4608 + (fb * 144 + fh * 80 + 4 * g) * 2;
  u32x2 z[NJ][2];
  float bs[2];
#pragma unroll
  for (int m = 0; m < 2; m++) {
    bs[m] = __builtin_bit_cast(float, (unsigned)(*(const unsigned short*)(b_sp + (trow + 16 * m) * 2)) << 16);
#pragma unroll
    for (int j = 0; j < NJ; j++) z[j][m] = *(const u32x2*)(ws + WS_Z1 + eo + (size_t)m * 16 * 4608 + j * 32);
  }
  khalf_exchange<NJ>(lds, w, acc);
  { char* ws_dbg = ws; TS(2, 3); }
#pragma unroll
  for (int m = 0; m < 2; m++)
#pragma unroll
    for (int j = 0; j < NJ; j++) {
      f32x4 a = acc[j][m];
      u32x2 o = {pk2(bflo(z[j][m][0]) * (a[0] + bs[m]), bfhi(z[j][m][0]) * (a[1] + bs[m])),
                 pk2(bflo(z[j][m][1]) * (a[2] + bs[m]), bfhi(z[j][m][1]) * (a[3] + bs[m]))};
      const int J = 9 * fb + 5 * fh + j;
      *(u32x2*)(ws + WS_H + (size_t)(8 * tb + 4 * th + 2 * kh + m) * 73728 + (J >> 1) * 1024 + l * 16 + (J & 1) * 8) = o;
    }
  { char* ws_dbg = ws; TS(2, 4); }
}
__global__ __launch_bounds__(512, 2) void k2_sgu(const char* __restrict__ w_sp, const char* __restrict__ b_sp,
                                                 char* __restrict__ ws) {
  extern __shared__ __attribute__((aligned(16))) char lds[];
  const int b = blockIdx.x, xcd = b & 7, i = b >> 3;
  const int tb = 8 * (xcd & 1) + (i & 7), fb = 4 * (xcd >> 1) + (i >> 3);
  const int w = __builtin_amdgcn_readfirstlane(threadIdx.x >> 6);
  if (w < 4) k2_body<5>(lds, w_sp, b_sp, ws, tb, fb, w);
  else k2_body<4>(lds, w_sp, b_sp, ws, tb, fb, w);
}

constexpr int KC_LDS = 8 * 20 * 1024;
struct FragC { u32x4 h[5]; u32x4 wv[4]; };

template <int NB, int KSTEP>
DEVI void kc_stream(const char* __restrict__ hb, const unsigned (&ho)[5], const char* __restrict__ wb, unsigned wofs,
                    int rot, f32x4 (&acc)[5][4]) {
#define KC_NBUF 3
  constexpr int NBUF = KC_NBUF;
  auto ld = [&](FragC& f, int i) {
    int ii = i + rot;
    if (ii >= NB) ii -= NB;
#pragma unroll
    for (int m = 0; m < 5; m++) f.h[m] = *(const u32x4*)(hb + (size_t)ii * (KSTEP * 1024) + ho[m]);
#pragma unroll
    for (int j = 0; j < 4; j++) f.wv[j] = *(const u32x4*)(wb + (size_t)ii * (KSTEP * 36 * 1024) + wofs + j * 1024);
    SB();
  };
  auto mm = [&](const FragC& f) {
#pragma unroll
    for (int m = 0; m < 5; m++)
#pragma unroll
      for (int j = 0; j < 4; j++)
        acc[m][j] = mfma(__builtin_bit_cast(bf16x8, f.wv[j]), __builtin_bit_cast(bf16x8, f.h[m]), acc[m][j]);
    SB();
  };
  FragC f[NBUF];
#pragma unroll
  for (int i = 0; i < NBUF - 1; i++) ld(f[i], i);
#pragma unroll
  for (int i = 0; i < NB; i++) {
    if (i + NBUF - 1 < NB) ld(f[(i + NBUF - 1) % NBUF], i + NBUF - 1);
    mm(f[i % NBUF]);
    if (i < NB - 1) bar();
    if (NB == 9 && (i == 1 || i == 4 || i == 6)) bar();
  }
}

__global__ __launch_bounds__(512, 2) void kc_out(const char* __restrict__ x, char* __restrict__ ws,
                                                 char* __restrict__ out) {
  extern __shared__ __attribute__((aligned(16))) char lds[];
  const int b = blockIdx.x, xcd = b & 7, i = b >> 3;
  const int unit = xcd * 30 + i;
  if (unit >= 234) return;
  const int tb = unit / 9, db = unit - tb * 9;
  const int l = threadIdx.x & 63, g = l >> 4, c16 = l & 15;
  const int w = __builtin_amdgcn_readfirstlane(threadIdx.x >> 6);
  char* ws_dbg = ws; TS(3, 0); TS(3, 1);
  f32x4 acc[5][4];
#pragma unroll
  for (int m = 0; m < 5; m++)
#pragma unroll
    for (int j = 0; j < 4; j++) acc[m][j] = (f32x4){0.f, 0.f, 0.f, 0.f};
  unsigned ho[5];
  const int rotH = 0, rotO = 0;
  if (w < 6) {
#pragma unroll
    for (int m = 0; m < 5; m++) ho[m] = min(5 * tb + m, 127) * 73728 + l * 16;
    kc_stream<12, 6>(ws + WS_H + w * 1024, ho, ws + WS_WF + (size_t)(w * 36 + db * 4) * 1024, l * 16, rotH, acc);
  } else {
#pragma unroll
    for (int m = 0; m < 5; m++) ho[m] = min(5 * tb + m, 127) * 18432 + l * 16;
    kc_stream<9, 2>(ws + WS_O + (w - 6) * 1024, ho, ws + WS_WF + (size_t)((72 + w - 6) * 36 + db * 4) * 1024, l * 16, rotO, acc);
  }
  TS(3, 2); TW(3, 0);
  u32x2 xv[3];
#pragma unroll
  for (int q = 0; q < 3; q++) {
    const int st = min(w + 8 * q, 19), ts = min(5 * tb + (st >> 2), 127);
    xv[q] = *(const u32x2*)(x + (size_t)(ts * 16 + c16) * 1152 + (db * 64 + (st & 3) * 16 + 4 * g) * 2);
  }
  f32x4* red = (f32x4*)lds;
#pragma unroll
  for (int m = 0; m < 5; m++)
#pragma unroll
    for (int j = 0; j < 4; j++) red[(w * 20 + m * 4 + j) * 64 + l] = acc[m][j];
  asm volatile("s_waitcnt lgkmcnt(0)" ::: "memory");
  TW(3, 1);
  lds_bar();
  TS(3, 3);
#pragma unroll
  for (int q = 0; q < 3; q++) {
    const int st = w + 8 * q, ts = 5 * tb + (st >> 2);
    if (st < 20 && ts < 128) {
      const f32x4* p = red + st * 64 + l;
      f32x4 y = (p[0] + p[20 * 64]) + (p[40 * 64] + p[60 * 64]) + (p[80 * 64] + p[100 * 64]);
      f32x4 sv = p[120 * 64] + p[140 * 64];
      const float xf[4] = {bflo(xv[q][0]), bfhi(xv[q][0]), bflo(xv[q][1]), bfhi(xv[q][1])};
      float o[4];
#pragma unroll
      for (int e = 0; e < 4; e++) {
        const float sig = __builtin_amdgcn_rcpf(1.0f + __builtin_amdgcn_exp2f(-1.4426950408889634f * y[e]));
        o[e] = xf[e] + sv[e] * (y[e] * sig);
      }
      *(u32x2*)(out + (size_t)(ts * 16 + c16) * 1152 + (db * 64 + (st & 3) * 16 + 4 * g) * 2) =
          (u32x2){pk2(o[0], o[1]), pk2(o[2], o[3])};
    }
  }
  TS(3, 4);
}

extern "C" void kernel_launch(void* const* d_in, const int* in_sizes, int n_in, void* d_out, int out_size,
                              void* d_ws, size_t ws_size, hipStream_t stream) {
  const char* x = (const char*)d_in[0];
  const char* wqkv = (const char*)d_in[1];
  const char* wo = (const char*)d_in[2];
  const char* w_in = (const char*)d_in[4];
  const char* w_sp = (const char*)d_in[5];
  const char* b_sp = (const char*)d_in[6];
  const char* w_out = (const char*)d_in[7];
  char* ws = (char*)d_ws;
  (void)hipFuncSetAttribute((const void*)k1_proj, hipFuncAttributeMaxDynamicSharedMemorySize, K1_LDS);
  (void)hipFuncSetAttribute((const void*)k_attn, hipFuncAttributeMaxDynamicSharedMemorySize, AT_LDS);
  (void)hipFuncSetAttribute((const void*)k2_sgu, hipFuncAttributeMaxDynamicSharedMemorySize, G_LDS);
  (void)hipFuncSetAttribute((const void*)kc_out, hipFuncAttributeMaxDynamicSharedMemorySize, KC_LDS);
  k0_ln<<<256 + 810, 64, 0, stream>>>(x, wo, w_out, ws);
  k1_proj<<<256, 512, K1_LDS, stream>>>(w_in, wqkv, ws);
  k_attn<<<256, 512, AT_LDS, stream>>>(ws);
  k2_sgu<<<256, 512, G_LDS, stream>>>(w_sp, b_sp, ws);
  kc_out<<<8 * 30, 512, KC_LDS, stream>>>(x, ws, (char*)d_out);
}
```

```cpp
#include <hip/hip_runtime.h>
#include <hip/hip_bf16.h>
#include <stdint.h>
#pragma clang diagnostic ignored "-Winline-asm"

typedef __attribute__((ext_vector_type(8))) __bf16 bf16x8;
typedef __attribute__((ext_vector_type(4))) __bf16 bf16x4;
typedef __attribute__((ext_vector_type(2))) __bf16 bf16x2;
typedef __attribute__((ext_vector_type(4))) float f32x4;
typedef __attribute__((ext_vector_type(2))) float f32x2;
typedef __attribute__((ext_vector_type(4))) unsigned u32x4;
typedef __attribute__((ext_vector_type(2))) unsigned u32x2;
typedef u32x4 u32x4_a4 __attribute__((aligned(4)));
typedef u32x2 u32x2_a4 __attribute__((aligned(4)));
#define AS1 __attribute__((address_space(1)))
#define AS3 __attribute__((address_space(3)))
#define DEVI __device__ __forceinline__

constexpr int S_ = 2048, D_ = 576, FH = 2304;

constexpr size_t WS_XN   = 0;
constexpr size_t WS_Z1   = WS_XN + (size_t)S_ * D_ * 2;
constexpr size_t WS_Z2   = WS_Z1 + (size_t)S_ * FH * 2;
constexpr size_t WS_H    = WS_Z2 + (size_t)S_ * FH * 2;
constexpr size_t WS_QKV  = WS_H + (size_t)S_ * FH * 2;
constexpr size_t WS_O    = WS_QKV + (size_t)96 * S_ * 18 * 2;
constexpr size_t WS_PART = WS_O + (size_t)S_ * D_ * 2;
constexpr size_t WS_STAT = WS_PART + (size_t)4 * S_ * D_ * 4;
constexpr size_t WS_WF   = WS_STAT + (size_t)16 * 16 * 128 * 8;

DEVI void glds16(const char* g, char* lds) {
  __builtin_amdgcn_global_load_lds((const AS1 void*)g, (AS3 void*)lds, 16, 0, 0);
}
DEVI void glds_s(unsigned voff, const char* sbase, unsigned ldsa) {
  asm volatile("s_mov_b32 m0, %2\n\ts_nop 0\n\tglobal_load_lds_dwordx4 %0, %1" ::"v"(voff), "s"(sbase), "s"(ldsa) : "memory", "m0");
}
DEVI void glds_s16(unsigned voff, const char* sbase, unsigned ldsa) {
  asm volatile("s_mov_b32 m0, %2\n\ts_mov_b64 exec, 0xffff\n\tglobal_load_lds_dwordx4 %0, %1\n\ts_mov_b64 exec, -1"
               ::"v"(voff), "s"(sbase), "s"(ldsa) : "memory", "m0");
}
DEVI void glds_s8(unsigned voff, const char* sbase, unsigned ldsa) {
  asm volatile("s_mov_b32 m0, %2\n\ts_mov_b64 exec, 0xff\n\tglobal_load_lds_dwordx4 %0, %1\n\ts_mov_b64 exec, -1"
               ::"v"(voff), "s"(sbase), "s"(ldsa) : "memory", "m0");
}
#define SB() __builtin_amdgcn_sched_barrier(0)
#define TS(K, I)
#define TW(K, I)
DEVI bf16x8 lds_b128(const char* p) { return *(const AS3 bf16x8*)p; }
DEVI bf16x4 lds_tr(const char* p) { return __builtin_amdgcn_ds_read_tr16_b64_v4bf16((AS3 bf16x4*)p); }
DEVI bf16x8 cat8(bf16x4 a, bf16x4 b) { return __builtin_shufflevector(a, b, 0, 1, 2, 3, 4, 5, 6, 7); }
DEVI f32x4 mfma(bf16x8 a, bf16x8 b, f32x4 c) { return __builtin_amdgcn_mfma_f32_16x16x32_bf16(a, b, c, 0, 0, 0); }
DEVI unsigned pk2(float a, float b) {
  f32x2 v = {a, b};
  bf16x2 r = __builtin_convertvector(v, bf16x2);
  return __builtin_bit_cast(unsigned, r);
}
DEVI float bflo(unsigned u) { return __builtin_bit_cast(float, u << 16); }
DEVI float bfhi(unsigned u) { return __builtin_bit_cast(float, u & 0xffff0000u); }
template <int N> DEVI void wait_vm_lgkm0() { asm volatile("s_waitcnt vmcnt(%0) lgkmcnt(0)" ::"n"(N) : "memory"); }
DEVI void bar() { __builtin_amdgcn_s_barrier(); asm volatile("" ::: "memory"); }
DEVI void lds_bar() { asm volatile("s_waitcnt lgkmcnt(0)" ::: "memory"); bar(); }
DEVI float gelu_t(float y) {
  float p = __builtin_fmaf(y * y, -0.1029432397f, -2.3022081983f);
  float e = __builtin_amdgcn_exp2f(y * p);
  return y * __builtin_amdgcn_rcpf(1.0f + e);
}
DEVI float fmx(float a, float b) { return __builtin_elementwise_maximum(a, b); }
DEVI int swap23(int r) { return (r & ~12) | ((r & 4) << 1) | ((r & 8) >> 1); }

DEVI void wprep(const char* __restrict__ wo, const char* __restrict__ w_out, char* __restrict__ ws, int kb, int dq, int l) {
  const int g = l >> 4, c16 = l & 15;
  unsigned short v[4][8];
#pragma unroll
  for (int e = 0; e < 8; e++) {
    const int r = e & 3, J = 2 * (kb < 72 ? kb : kb - 72) + (e >> 2);
    const char* src;
    if (kb < 72) src = w_out + (size_t)(16 * J + 4 * g + r) * 1152;
    else if (J < 32) src = wo + (size_t)(18 * J + 4 * g + r) * 1152;
    else src = wo + (size_t)((8 * (J - 32) + 2 * g + (r >> 1)) * 18 + 16 + (r & 1)) * 1152;
#pragma unroll
    for (int j = 0; j < 4; j++) v[j][e] = *(const unsigned short*)(src + (dq * 64 + j * 16 + c16) * 2);
  }
#pragma unroll
  for (int j = 0; j < 4; j++) {
    u32x4 o;
#pragma unroll
    for (int e = 0; e < 4; e++) o[e] = (unsigned)v[j][2 * e] | ((unsigned)v[j][2 * e + 1] << 16);
    *(u32x4*)(ws + WS_WF + ((size_t)kb * 36 + dq * 4 + j) * 1024 + l * 16) = o;
  }
}

__global__ __launch_bounds__(64) void k0_ln(const char* __restrict__ x, const char* __restrict__ wo,
                                            const char* __restrict__ w_out, char* __restrict__ ws) {
  const int l = threadIdx.x, b = blockIdx.x;
  if (b >= 256) { wprep(wo, w_out, ws, (b - 256) / 9, (b - 256) % 9, l); return; }
  const int row = b * 8 + (l >> 3), c0 = l & 7;
  char* ws_dbg = ws; TS(0, 0);
  const char* src = x + (size_t)row * 1152 + c0 * 16;
  u32x4 v[9];
#pragma unroll
  for (int i = 0; i < 9; i++) v[i] = *(const u32x4*)(src + i * 128);
  *(u32x4*)(ws + WS_STAT + (size_t)b * 1024 + l * 16) = (u32x4){~0u, ~0u, ~0u, ~0u};
  float s = 0.f;
#pragma unroll
  for (int i = 0; i < 9; i++)
#pragma unroll
    for (int e = 0; e < 4; e++) s += bflo(v[i][e]) + bfhi(v[i][e]);
  s += __shfl_xor(s, 1); s += __shfl_xor(s, 2); s += __shfl_xor(s, 4);
  const float mean = s * (1.0f / 576.0f);
  float q = 0.f;
#pragma unroll
  for (int i = 0; i < 9; i++)
#pragma unroll
    for (int e = 0; e < 4; e++) {
      float a = bflo(v[i][e]) - mean, c = bfhi(v[i][e]) - mean;
      q += a * a + c * c;
    }
  q += __shfl_xor(q, 1); q += __shfl_xor(q, 2); q += __shfl_xor(q, 4);
  const float rs = 1.0f / __builtin_sqrtf(q * (1.0f / 576.0f) + 1e-5f);
  const int r = row & 127;
  char* dst = ws + WS_XN + (size_t)((row >> 7) * 18 + (c0 >> 2)) * 8192 + r * 64 + (((c0 & 3) ^ ((-(r >> 2)) & 3)) * 16);
#pragma unroll
  for (int i = 0; i < 9; i++) {
    u32x4 o;
#pragma unroll
    for (int e = 0; e < 4; e++) o[e] = pk2((bflo(v[i][e]) - mean) * rs, (bfhi(v[i][e]) - mean) * rs);
    *(u32x4*)(dst + i * 16384) = o;
  }
  TS(0, 4);
}

constexpr int K1_SLOT = 8192 + 25600;
constexpr int K1_STAT = 4 * K1_SLOT;
constexpr int K1_STAT2 = K1_STAT + 4096;
constexpr int K1_LDS = K1_STAT2 + 4096;
constexpr int K1_QIMG = 0;
constexpr int K1_ZIMG = 32768;

template <int NJ> struct Frag1 { bf16x8 a[NJ]; bf16x8 b[4]; };

struct K1Stage {
  unsigned xo, wo[4], xdst, wdst;
  const char* Xb;
  const char* Wa;
  const char* Wc;
  const char* Wq;
  unsigned cstep;
  DEVI void init(char* lds, const char* w_in, const char* wqkv, const char* ws, int rb, int cg, int w) {
    const int l = threadIdx.x & 63;
    const int qstart = 108 * cg - ((cg & 1) ? 4 : 0);
    Xb = ws + WS_XN + (size_t)rb * 18 * 8192;
    xo = w * 1024 + l * 16;
#pragma unroll
    for (int i = 0; i < 4; i++) {
      const int ins = (i < 3) ? w + 8 * i : 24;
      if (ins < 18) {
        const int ty = ins >= 9, q = (ins - 9 * ty) * 64 + l;
        const int rho = q / 18, c = q - rho * 18;
        wo[i] = swap23(rho) * 9216 + ((ty ? 0 : FH) + 144 * cg + c * 8) * 2;
      } else {
        const int q = (i < 3) ? (ins - 18) * 64 + l : 6 * 64 + w * 8 + (l & 7);
        const int rho = q / 14, c = q - rho * 14;
        wo[i] = swap23(rho) * 3456 + (qstart + c * 8) * 2;
      }
    }
    Wa = w_in; Wq = wqkv;
    Wc = (w < 2) ? w_in : wqkv;
    cstep = (w < 2) ? 32 * 9216 : 32 * 3456;
    const unsigned lb = (unsigned)(uintptr_t)(AS3 char*)lds;
    xdst = lb + w * 1024;
    wdst = lb + 8192 + w * 1024;
  }
  DEVI void piece(int t, int i, int w) const {
    const unsigned so = (t & 3) * K1_SLOT;
    if (i == 0) glds_s(xo, Xb + t * 8192, xdst + so);
    else if (i == 1) glds_s(wo[0], Wa + (size_t)t * (32 * 9216), wdst + so);
    else if (i == 2) glds_s(wo[1], Wa + (size_t)t * (32 * 9216), wdst + so + 8192);
    else if (i == 3) glds_s(wo[2], Wc + (size_t)t * cstep, wdst + so + 16384);
    else glds_s8(wo[3], Wq + (size_t)t * (32 * 3456), wdst + so + 24576 - w * 896);
  }
  DEVI void issue(int t, int w) const {
#pragma unroll
    for (int i = 0; i < 5; i++) piece(t, i, w);
  }
};

DEVI f32x2 gelu2(f32x2 y) {
  const f32x2 c1 = {-0.1029432397f, -0.1029432397f}, c0 = {-2.3022081983f, -2.3022081983f};
  const f32x2 a = y * __builtin_elementwise_fma(y * y, c1, c0);
  const float a0 = a[0], a1 = a[1];
  const f32x2 d = (f32x2){__builtin_amdgcn_exp2f(a0), __builtin_amdgcn_exp2f(a1)} + (f32x2){1.0f, 1.0f};
  const float d0 = d[0], d1 = d[1];
  return y * (f32x2){__builtin_amdgcn_rcpf(d0), __builtin_amdgcn_rcpf(d1)};
}
DEVI unsigned pk2v(f32x2 v) { return __builtin_bit_cast(unsigned, __builtin_convertvector(v, bf16x2)); }
DEVI f32x2 lo2(f32x4 v) { return __builtin_shufflevector(v, v, 0, 1); }
DEVI f32x2 hi2(f32x4 v) { return __builtin_shufflevector(v, v, 2, 3); }

template <int NQ>
DEVI void k1_body(char* lds, const K1Stage& gs, char* __restrict__ ws, int rb, int cg, int w, int sh) {
  constexpr int NJ = (NQ == 0) ? 7 : 6;
  constexpr int NZ2 = (NQ == 0) ? 3 : 2;
  constexpr int NZ1 = (NQ == 1) ? 3 : 2;
  constexpr int Z2I = (NQ == 0) ? 0 : (NQ == 1) ? 3 : (NQ == 2) ? 5 : 7;
  constexpr int Z1I = (NQ == 0) ? 0 : (NQ == 1) ? 2 : (NQ == 2) ? 5 : 7;
  constexpr int QI  = (NQ == 0) ? 0 : (NQ == 1) ? 2 : (NQ == 2) ? 3 : 5;
  constexpr int NS = 18;
  char* ws_dbg = ws;
  const int tid = threadIdx.x, l = tid & 63, g = l >> 4, c16 = l & 15;
  const int qstart = 108 * cg - ((cg & 1) ? 4 : 0);

  const int boff = (64 * sh + c16) * 64 + ((g ^ ((-(c16 >> 2)) & 3)) * 16);
  const int rowp = 16 * (g >> 1) + 4 * (g & 1) + (c16 >> 2);
  const int aoffz = 8192 + rowp * 288 + 8 * (c16 & 3);
  const int aoffq = 8192 + 18432 + rowp * 224 + 8 * (c16 & 3);
  auto rda = [&](const char* slot, int j, int h) -> bf16x4 {
    if (j < NZ2) return lds_tr(slot + aoffz + (Z2I + j) * 32 + h * 8 * 288);
    if (j < NZ2 + NZ1) return lds_tr(slot + aoffz + 9216 + (Z1I + j - NZ2) * 32 + h * 8 * 288);
    return lds_tr(slot + aoffq + (QI + j - NZ2 - NZ1) * 32 + h * 8 * 224);
  };
  auto rd = [&](Frag1<NJ>& f, int t) {
    const char* slot = lds + (t & 3) * K1_SLOT;
#pragma unroll
    for (int m = 0; m < 4; m++) f.b[m] = lds_b128(slot + boff + m * 1024);
#pragma unroll
    for (int j = 0; j < NJ; j++) f.a[j] = cat8(rda(slot, j, 0), rda(slot, j, 1));
  };
  f32x4 acc[NJ][4];
#pragma unroll
  for (int j = 0; j < NJ; j++)
#pragma unroll
    for (int m = 0; m < 4; m++) acc[j][m] = (f32x4){0.f, 0.f, 0.f, 0.f};
  auto step = [&](const Frag1<NJ>& cur, Frag1<NJ>& nxt, int t, bool doissue) {
    const char* slot = lds + ((t + 1) & 3) * K1_SLOT;
    bf16x4 lo[NJ];
#pragma unroll
    for (int x = 0; x < 4 * NJ; x++) {
      const int j = x >> 2, m = x & 3;
      acc[j][m] = mfma(cur.a[j], cur.b[m], acc[j][m]);
      if (x < 4) nxt.b[x] = lds_b128(slot + boff + x * 1024);
      else if (x < 4 + 2 * NJ) {
        const int y = x - 4, jj = y >> 1;
        if ((y & 1) == 0) lo[jj] = rda(slot, jj, 0);
        else nxt.a[jj] = cat8(lo[jj], rda(slot, jj, 1));
      }
      if (doissue && (x & 3) == 1 && (x >> 2) < 5) gs.piece(t + 4, x >> 2, w);
      SB();
    }
  };
  Frag1<NJ> f0, f1;
  wait_vm_lgkm0<15>();
  bar();
  TS(1, 1);
  rd(f0, 0);
#pragma unroll 1
  for (int t = 0; t < NS - 4; t += 2) {
    wait_vm_lgkm0<10>(); bar(); SB();
    step(f0, f1, t, true);
    wait_vm_lgkm0<10>(); bar(); SB();
    step(f1, f0, t + 1, true);
  }
  wait_vm_lgkm0<10>(); bar(); SB(); step(f0, f1, NS - 4, false);
  wait_vm_lgkm0<5>();  bar(); SB(); step(f1, f0, NS - 3, false);
  wait_vm_lgkm0<0>();  bar(); SB(); step(f0, f1, NS - 2, false);
#pragma unroll
  for (int j = 0; j < NJ; j++)
#pragma unroll
    for (int m = 0; m < 4; m++) acc[j][m] = mfma(f1.a[j], f1.b[m], acc[j][m]);
  TS(1, 2);
  {
    f32x2* st = (f32x2*)(lds + K1_STAT) + NQ * 128 + 64 * sh + c16;
#pragma unroll
    for (int m = 0; m < 4; m++) {
      f32x2 a2 = {0.f, 0.f}, q2 = a2;
#pragma unroll
      for (int j = 0; j < NZ2; j++) {
        const f32x2 u = gelu2(lo2(acc[j][m])), v = gelu2(hi2(acc[j][m]));
        acc[j][m] = __builtin_shufflevector(u, v, 0, 1, 2, 3);
        a2 += u + v;
        q2 = __builtin_elementwise_fma(u, u, __builtin_elementwise_fma(v, v, q2));
      }
      float a = a2[0] + a2[1], q = q2[0] + q2[1];
      a += __shfl_xor(a, 16); q += __shfl_xor(q, 16);
      a += __shfl_xor(a, 32); q += __shfl_xor(q, 32);
      if (g == 0) st[16 * m] = (f32x2){a, q};
    }
  }
  lds_bar();
  AS1 unsigned long long* gstat = (AS1 unsigned long long*)(ws + WS_STAT) + (size_t)rb * 16 * 128;
  if (tid < 128) {
    const f32x2* st = (const f32x2*)(lds + K1_STAT) + tid;
    f32x2 v = st[0] + st[128] + st[256] + st[384];
    const float v0 = v[0], v1 = v[1];
    unsigned lo = __builtin_bit_cast(unsigned, v0), hi = __builtin_bit_cast(unsigned, v1);
    if (hi == 0xffffffffu) hi = 0x7fc00000u;
    __hip_atomic_store(gstat + cg * 128 + tid, ((unsigned long long)hi << 32) | lo, __ATOMIC_RELAXED,
                       __HIP_MEMORY_SCOPE_AGENT);
  }
#pragma unroll
  for (int j = NZ2 + NZ1; j < NJ; j++) {
#pragma unroll
    for (int p = 0; p < 2; p++) {
      const int c = (QI + (j - NZ2 - NZ1)) * 16 + 4 * g + 2 * p + qstart - 108 * cg;
      const int hp = (c * 3641) >> 16, d = c - hp * 18;
      char* dst = lds + K1_QIMG + hp * 4608 + (64 * sh + c16) * 36 + d * 2;
      if (c >= 0 && c < 108) {
#pragma unroll
        for (int m = 0; m < 4; m++) *(unsigned*)(dst + m * 16 * 36) = pk2(acc[j][m][2 * p], acc[j][m][2 * p + 1]);
      }
    }
  }
#pragma unroll
  for (int j = 0; j < NZ1; j++)
#pragma unroll
    for (int mp = 0; mp < 2; mp++) {
      const f32x4 u = acc[NZ2 + j][2 * mp], v = acc[NZ2 + j][2 * mp + 1];
      const u32x4 o = {pk2v(gelu2(lo2(u))), pk2v(gelu2(hi2(u))), pk2v(gelu2(lo2(v))), pk2v(gelu2(hi2(v)))};
      *(u32x4*)(ws + WS_Z1 + ((size_t)(4 * rb + 2 * sh + mp) * 144 + 9 * cg + Z1I + j) * 1024 + l * 16) = o;
    }
  const int prow = tid & 127, part = tid >> 7;
  unsigned long long sv[4];
#pragma unroll
  for (int i = 0; i < 4; i++)
    sv[i] = __hip_atomic_load(gstat + (part * 4 + i) * 128 + prow, __ATOMIC_RELAXED, __HIP_MEMORY_SCOPE_AGENT);
  lds_bar();
  TS(1, 3);
#pragma unroll
  for (int u = 0; u < 4; u++) {
    const int q = tid + 512 * u;
    if (u < 3 || q < 1728) {
      const int pi = (q * 3641) >> 20, rem = q - pi * 288;
      *(u32x4*)(ws + WS_QKV + ((size_t)(6 * cg + pi) * S_ + rb * 128) * 36 + rem * 16) =
          *(const u32x4*)(lds + K1_QIMG + q * 16);
    }
  }
  {
    float a = 0.f, q = 0.f;
#pragma unroll
    for (int i = 0; i < 4; i++) {
      unsigned long long v = sv[i];
      while ((unsigned)(v >> 32) == 0xffffffffu)
        v = __hip_atomic_load(gstat + (part * 4 + i) * 128 + prow, __ATOMIC_RELAXED, __HIP_MEMORY_SCOPE_AGENT);
      a += __builtin_bit_cast(float, (unsigned)v);
      q += __builtin_bit_cast(float, (unsigned)(v >> 32));
    }
    ((f32x2*)(lds + K1_STAT2))[part * 128 + prow] = (f32x2){a, q};
  }
  lds_bar();
#pragma unroll
  for (int m = 0; m < 4; m++) {
    const f32x2* st = (const f32x2*)(lds + K1_STAT2) + 64 * sh + 16 * m + c16;
    f32x2 v = st[0] + st[128] + st[256] + st[384];
    const float mean = v[0] * (1.0f / FH);
    const float var = fmaxf(v[1] * (1.0f / FH) - mean * mean, 0.f);
    const float rs = 1.0f / __builtin_sqrtf(var + 1e-5f);
    const f32x2 mean2 = {mean, mean}, rs2 = {rs, rs};
    char* dst = lds + K1_ZIMG + (64 * sh + 16 * m + swap23(c16)) * 288 + Z2I * 32 + g * 8;
#pragma unroll
    for (int j = 0; j < NZ2; j++)
      *(u32x2*)(dst + j * 32) = (u32x2){pk2v((lo2(acc[j][m]) - mean2) * rs2), pk2v((hi2(acc[j][m]) - mean2) * rs2)};
  }
  lds_bar();
  {
    char* zt = ws + WS_Z2 + (size_t)cg * 589824 + (size_t)rb * 36864;
#pragma unroll
    for (int u = 0; u < 5; u++) {
      const int q = tid + 512 * u;
      if (u < 4 || q < 2304) *(u32x4*)(zt + q * 16) = *(const u32x4*)(lds + K1_ZIMG + q * 16);
    }
  }
  TS(1, 4);
}

__global__ __launch_bounds__(512, 2) void k1_proj(const char* __restrict__ w_in, const char* __restrict__ wqkv,
                                                  char* __restrict__ ws) {
  extern __shared__ __attribute__((aligned(16))) char lds[];
  const int b = blockIdx.x, xcd = b & 7, i = b >> 3;
  const int rb = 8 * (xcd & 1) + (i & 7), cg = 4 * (xcd >> 1) + (i >> 3);
  const int w = __builtin_amdgcn_readfirstlane(threadIdx.x >> 6);
  const int sh = w >> 2, nq = sh ? 3 - (w & 3) : (w & 3);
  char* ws_dbg = ws; TS(1, 0);
  K1Stage gs;
  gs.init(lds, w_in, wqkv, ws, rb, cg, w);
  gs.issue(0, w); gs.issue(1, w); gs.issue(2, w); gs.issue(3, w);
  if (nq == 0) k1_body<0>(lds, gs, ws, rb, cg, w, sh);
  else if (nq == 1) k1_body<1>(lds, gs, ws, rb, cg, w, sh);
  else if (nq == 2) k1_body<2>(lds, gs, ws, rb, cg, w, sh);
  else k1_body<3>(lds, gs, ws, rb, cg, w, sh);
}

template <class Hook>
DEVI void attn_phase(char* lds, char* __restrict__ ws, Hook hook) {
  const int b = blockIdx.x, h = b & 31, qb = b >> 5;
  const int tid = threadIdx.x, l = tid & 63, g = l >> 4, c16 = l & 15;
  const int w = __builtin_amdgcn_readfirstlane(tid >> 6);
  const int q0 = qb * 256;
  char* ws_dbg = ws;
  const char* qkv = ws + WS_QKV;
  const int key = q0 - 256 + tid;
  u32x4 qv[2];
#pragma unroll
  for (int m = 0; m < 2; m++)
    qv[m] = *(const u32x4_a4*)(qkv + ((size_t)h * S_ + q0 + 32 * w + 16 * m + c16) * 36 + (g < 2 ? 16 * g : 20));
  const char* kp = qkv + ((size_t)(32 + h) * S_ + max(key, 0)) * 36;
  const char* vp = kp + (size_t)32 * S_ * 36;
  const u32x4 k0 = *(const u32x4_a4*)kp, k1 = *(const u32x4_a4*)(kp + 16);
  const unsigned k2 = *(const unsigned*)(kp + 32);
  const u32x4 v0 = *(const u32x4_a4*)vp, v1 = *(const u32x4_a4*)(vp + 16);
  const unsigned v2 = *(const unsigned*)(vp + 32);
  bf16x8 qf[2];
  {
    const int f = (-(tid >> 2)) & 3;
    char* kr = lds + tid * 64;
    *(u32x4*)(kr + ((0 ^ f) * 16)) = k0;
    *(u32x4*)(kr + ((1 ^ f) * 16)) = k1;
    *(u32x4*)(kr + ((2 ^ f) * 16)) = (u32x4){k2, key < 0 ? 0x0000f149u : 0u, 0, 0};
    *(u32x4*)(kr + ((3 ^ f) * 16)) = (u32x4){0, 0, 0, 0};
    char* vr = lds + 32768 + tid * 64;
    const int x = ((tid >> 2) & 1) * 32;
    *(u32x4*)(vr + (0 ^ x)) = v0;
    *(u32x4*)(vr + (16 ^ x)) = v1;
    *(u32x4*)(vr + (32 ^ x)) = (u32x4){v2, 0x00003f80u, 0, 0};
    *(u32x4*)(vr + (48 ^ x)) = (u32x4){0, 0, 0, 0};
  }
#pragma unroll
  for (int m = 0; m < 2; m++) {
    u32x4 v = qv[m];
    if (g >= 2) v = (u32x4){g == 2 ? v[3] : 0u, g == 2 ? 0x00003f80u : 0u, 0, 0};
    qf[m] = __builtin_bit_cast(bf16x8, v);
  }
  hook();
  lds_bar();
  TW(2, 0);
  const float sc = 0.23570226039551584f * 1.4426950408889634f;
  const int koff = (32 * w + c16) * 64 + ((g ^ ((-(c16 >> 2)) & 3)) * 16);
  const int voff = 32768 + (32 * w + 4 * g + (c16 >> 2)) * 64 + 8 * (c16 & 3);
  const int vx = (g & 1) * 32;
#pragma unroll
  for (int m = 0; m < 2; m++) {
    f32x4 s[17];
#pragma unroll
    for (int i = 0; i < 17; i++) {
      bf16x8 kf = lds_b128(lds + koff + (i + m) * 1024);
      s[i] = mfma(kf, qf[m], (f32x4){0.f, 0.f, 0.f, 0.f});
    }
    const float ninf = -__builtin_inff();
#pragma unroll
    for (int r = 0; r < 4; r++) {
      if (!(c16 < 4 * g + r)) s[0][r] = ninf;
      if (!(4 * g + r <= c16)) s[16][r] = ninf;
    }
    float mxa = ninf, mxb = ninf;
#pragma unroll
    for (int i = 0; i < 17; i++) { mxa = fmx(fmx(mxa, s[i][0]), s[i][1]); mxb = fmx(fmx(mxb, s[i][2]), s[i][3]); }
    float mx = fmx(mxa, mxb);
    mx = fmx(mx, __shfl_xor(mx, 16));
    mx = fmx(mx, __shfl_xor(mx, 32));
    const float nb = -mx * sc;
    const f32x2 sc2 = {sc, sc}, nb2 = {nb, nb};
    unsigned p[18][2];
#pragma unroll
    for (int i = 0; i < 17; i++) {
      const f32x2 ta = __builtin_elementwise_fma((f32x2){s[i][0], s[i][1]}, sc2, nb2);
      const f32x2 tb = __builtin_elementwise_fma((f32x2){s[i][2], s[i][3]}, sc2, nb2);
      const float a0 = ta[0], a1 = ta[1], b0 = tb[0], b1 = tb[1];
      p[i][0] = pk2(__builtin_amdgcn_exp2f(a0), __builtin_amdgcn_exp2f(a1));
      p[i][1] = pk2(__builtin_amdgcn_exp2f(b0), __builtin_amdgcn_exp2f(b1));
    }
    p[17][0] = 0; p[17][1] = 0;
    f32x4 o0 = {0.f, 0.f, 0.f, 0.f}, o1 = o0;
#pragma unroll
    for (int i = 0; i < 9; i++) {
      const int r0 = (2 * i + m) * 1024, r1 = (i < 8) ? r0 + 1024 : r0;
      bf16x8 pf = __builtin_bit_cast(bf16x8, (u32x4){p[2 * i][0], p[2 * i][1], p[2 * i + 1][0], p[2 * i + 1][1]});
      bf16x8 va = cat8(lds_tr(lds + voff + vx + r0), lds_tr(lds + voff + vx + r1));
      bf16x8 vb = cat8(lds_tr(lds + voff + (32 ^ vx) + r0), lds_tr(lds + voff + (32 ^ vx) + r1));
      o0 = mfma(va, pf, o0);
      o1 = mfma(vb, pf, o1);
    }
    const float den = __shfl(o1[2], c16);
    const float inv = 1.0f / den;
    char* op = ws + WS_O + (size_t)(16 * qb + 2 * w + m) * 18432;
    *(u32x2*)(op + (h >> 1) * 1024 + l * 16 + (h & 1) * 8) = (u32x2){pk2(o0[0] * inv, o0[1] * inv), pk2(o0[2] * inv, o0[3] * inv)};
    if (g == 0)
      *(unsigned*)(op + (16 + (h >> 4)) * 1024 + (((h & 7) >> 1) * 16 + c16) * 16 + ((h >> 3) & 1) * 8 + (h & 1) * 4) =
          pk2(o1[0] * inv, o1[1] * inv);
  }
  TW(2, 1);
}

constexpr int G_SLOT = 16384 + 18432;
constexpr int G_LDS = 4 * G_SLOT;
template <int NJ> struct Frag2 { bf16x8 a[NJ]; bf16x8 b[4]; };

struct GStage {
  unsigned bo[2], ao[3], bdst, adst;
  const char* Bb;
  const char* Ab;
  DEVI void init(char* lds, const char* B, const char* A, int w) {
    const int l = threadIdx.x & 63;
    Bb = B; Ab = A;
#pragma unroll
    for (int i = 0; i < 2; i++) {
      int r = (2 * w + i) * 8 + (l >> 3);
      int c = (l & 7) ^ ((r >> 1) & 7);
      bo[i] = r * 4096 + c * 16;
    }
#pragma unroll
    for (int i = 0; i < 3; i++) {
      int q = w * 144 + i * 64 + ((i == 2) ? (l & 15) : l);
      ao[i] = q * 16;
    }
    const unsigned lb = (unsigned)(uintptr_t)(AS3 char*)lds;
    bdst = lb + w * 2048; adst = lb + 16384 + w * 2304;
  }
  DEVI void piece(int t, int i) const {
    const unsigned so = (t & 3) * G_SLOT;
    const size_t astep = 18432;
    if (i < 2) glds_s(bo[i], Bb + t * 128, bdst + so + i * 1024);
    else if (i < 4) glds_s(ao[i - 2], Ab + (size_t)t * astep, adst + so + (i - 2) * 1024);
    else glds_s16(ao[2], Ab + (size_t)t * astep, adst + so + 2048);
  }
  DEVI void issue(int t) const {
#pragma unroll
    for (int i = 0; i < 5; i++) piece(t, i);
  }
};

template <int NJ, int NS>
DEVI void gemm_core(char* lds, const GStage& gs, int w, f32x4 (&acc)[NJ][4], char* ws_dbg, int kid) {
  const int l = threadIdx.x & 63, g = l >> 4, c16 = l & 15;
  const int fh = w >> 2, th = w & 1, kh = (w >> 1) & 1;
  const int boff = (64 * th + c16) * 128 + (((4 * kh + g) ^ (c16 >> 1)) * 16);
  const int aoff = 16384 + (32 * kh + 16 * (g >> 1) + 4 * (g & 1) + (c16 >> 2)) * 288 + fh * 160 + 8 * (c16 & 3);
  auto rd = [&](Frag2<NJ>& f, int t) {
    const char* slot = lds + (t & 3) * G_SLOT;
#pragma unroll
    for (int m = 0; m < 4; m++) f.b[m] = lds_b128(slot + boff + m * 2048);
#pragma unroll
    for (int j = 0; j < NJ; j++)
      f.a[j] = cat8(lds_tr(slot + aoff + j * 32), lds_tr(slot + aoff + j * 32 + 8 * 288));
  };
  auto step = [&](const Frag2<NJ>& cur, Frag2<NJ>& nxt, int t, bool doissue) {
    const char* slot = lds + ((t + 1) & 3) * G_SLOT;
    const char* pb = slot + boff;
    const char* pa = slot + aoff;
    bf16x4 lo[NJ];
#pragma unroll
    for (int x = 0; x < 4 * NJ; x++) {
      const int j = x >> 2, m = x & 3;
      acc[j][m] = mfma(cur.a[j], cur.b[m], acc[j][m]);
      if (x < 4) nxt.b[x] = lds_b128(pb + x * 2048);
      else if (x < 4 + 2 * NJ) {
        const int y = x - 4, jj = y >> 1;
        if ((y & 1) == 0) lo[jj] = lds_tr(pa + jj * 32);
        else nxt.a[jj] = cat8(lo[jj], lds_tr(pa + jj * 32 + 8 * 288));
      }
      if (doissue && (x % 3) == 1 && x / 3 < 5) gs.piece(t + 4, x / 3);
      SB();
    }
  };
  auto mm = [&](const Frag2<NJ>& f) {
#pragma unroll
    for (int j = 0; j < NJ; j++)
#pragma unroll
      for (int m = 0; m < 4; m++) acc[j][m] = mfma(f.a[j], f.b[m], acc[j][m]);
  };
#pragma unroll
  for (int j = 0; j < NJ; j++)
#pragma unroll
    for (int m = 0; m < 4; m++) acc[j][m] = (f32x4){0.f, 0.f, 0.f, 0.f};
  Frag2<NJ> f0, f1;
  wait_vm_lgkm0<15>();
  bar();
  TS(kid, 1);
  rd(f0, 0);
  constexpr int M = NS - 4, MP = M & ~1;
#pragma unroll 1
  for (int t = 0; t < MP; t += 2) {
    wait_vm_lgkm0<10>(); bar(); SB();
    step(f0, f1, t, true);
    wait_vm_lgkm0<10>(); bar(); SB();
    step(f1, f0, t + 1, true);
  }
  if constexpr (M & 1) {
    wait_vm_lgkm0<10>(); bar(); SB(); step(f0, f1, NS - 5, true);
    wait_vm_lgkm0<10>(); bar(); SB(); step(f1, f0, NS - 4, false);
    wait_vm_lgkm0<5>();  bar(); SB(); step(f0, f1, NS - 3, false);
    wait_vm_lgkm0<0>();  bar(); SB(); step(f1, f0, NS - 2, false);
    mm(f0);
  } else {
    wait_vm_lgkm0<10>(); bar(); SB(); step(f0, f1, NS - 4, false);
    wait_vm_lgkm0<5>();  bar(); SB(); step(f1, f0, NS - 3, false);
    wait_vm_lgkm0<0>();  bar(); SB(); step(f0, f1, NS - 2, false);
    mm(f1);
  }
  TS(kid, 2);
}

template <int NJ>
DEVI void khalf_exchange(char* lds, int w, f32x4 (&acc)[NJ][4]) {
  const int l = threadIdx.x & 63, kh = (w >> 1) & 1;
  lds_bar();
  f32x4* mine = (f32x4*)(lds + w * 10240) + l;
  const f32x4* other = (const f32x4*)(lds + (w ^ 2) * 10240) + l;
  if (kh == 0) {
#pragma unroll
    for (int j = 0; j < NJ; j++) { mine[(2 * j) * 64] = acc[j][2]; mine[(2 * j + 1) * 64] = acc[j][3]; }
  } else {
#pragma unroll
    for (int j = 0; j < NJ; j++) {
      mine[(2 * j) * 64] = acc[j][0]; mine[(2 * j + 1) * 64] = acc[j][1];
      acc[j][0] = acc[j][2]; acc[j][1] = acc[j][3];
    }
  }
  lds_bar();
#pragma unroll
  for (int j = 0; j < NJ; j++) { acc[j][0] += other[(2 * j) * 64]; acc[j][1] += other[(2 * j + 1) * 64]; }
}

template <int NJ>
DEVI void k2_body(char* lds, const GStage& gs, const char* __restrict__ b_sp, char* __restrict__ ws,
                  int tb, int fb, int w) {
  f32x4 acc[NJ][4];
  gemm_core<NJ, 32>(lds, gs, w, acc, ws, 2);
  const int l = threadIdx.x & 63, g = l >> 4, c16 = l & 15;
  const int fh = w >> 2, th = w & 1, kh = (w >> 1) & 1;
  const int trow = tb * 128 + 64 * th + 32 * kh + c16;
  u32x4 z[NJ];
  float bs[2];
#pragma unroll
  for (int m = 0; m < 2; m++)
    bs[m] = __builtin_bit_cast(float, (unsigned)(*(const unsigned short*)(b_sp + (trow + 16 * m) * 2)) << 16);
#pragma unroll
  for (int j = 0; j < NJ; j++)
    z[j] = *(const u32x4*)(ws + WS_Z1 + ((size_t)(4 * tb + 2 * th + kh) * 144 + 9 * fb + 5 * fh + j) * 1024 + l * 16);
  khalf_exchange<NJ>(lds, w, acc);
  { char* ws_dbg = ws; TS(2, 3); }
#pragma unroll
  for (int m = 0; m < 2; m++)
#pragma unroll
    for (int j = 0; j < NJ; j++) {
      f32x4 a = acc[j][m];
      const unsigned z0 = z[j][2 * m], z1 = z[j][2 * m + 1];
      u32x2 o = {pk2(bflo(z0) * (a[0] + bs[m]), bfhi(z0) * (a[1] + bs[m])),
                 pk2(bflo(z1) * (a[2] + bs[m]), bfhi(z1) * (a[3] + bs[m]))};
      const int J = 9 * fb + 5 * fh + j;
      *(u32x2*)(ws + WS_H + (size_t)(8 * tb + 4 * th + 2 * kh + m) * 73728 + (J >> 1) * 1024 + l * 16 + (J & 1) * 8) = o;
    }
  { char* ws_dbg = ws; TS(2, 4); }
}
__global__ __launch_bounds__(512, 2) void k2_sgu(const char* __restrict__ w_sp, const char* __restrict__ b_sp,
                                                 char* __restrict__ ws) {
  extern __shared__ __attribute__((aligned(16))) char lds[];
  const int b = blockIdx.x, xcd = b & 7, i = b >> 3;
  const int tb = 8 * (xcd & 1) + (i & 7), fb = 4 * (xcd >> 1) + (i >> 3);
  const int w = __builtin_amdgcn_readfirstlane(threadIdx.x >> 6);
  char* ws_dbg = ws; TS(2, 0);
  GStage gs;
  gs.init(lds, w_sp + (size_t)tb * 128 * 4096, ws + WS_Z2 + (size_t)fb * 589824, w);
  attn_phase(lds + 2 * G_SLOT, ws, [&]() { gs.issue(0); gs.issue(1); });
  lds_bar();
  gs.issue(2); gs.issue(3);
  if (w < 4) k2_body<5>(lds, gs, b_sp, ws, tb, fb, w);
  else k2_body<4>(lds, gs, b_sp, ws, tb, fb, w);
}

constexpr int KC_LDS = 8 * 20 * 1024;
struct FragC { u32x4 h[5]; u32x4 wv[4]; };

template <int NB, int KSTEP>
DEVI void kc_stream(const char* __restrict__ hb, const unsigned (&ho)[5], const char* __restrict__ wb, unsigned wofs,
                    f32x4 (&acc)[5][4]) {
  constexpr int NBUF = 3;
  auto ld = [&](FragC& f, int i) {
    const int ii = i;
#pragma unroll
    for (int m = 0; m < 5; m++) f.h[m] = *(const u32x4*)(hb + (size_t)ii * (KSTEP * 1024) + ho[m]);
#pragma unroll
    for (int j = 0; j < 4; j++) f.wv[j] = *(const u32x4*)(wb + (size_t)ii * (KSTEP * 36 * 1024) + wofs + j * 1024);
    SB();
  };
  auto mm = [&](const FragC& f) {
#pragma unroll
    for (int m = 0; m < 5; m++)
#pragma unroll
      for (int j = 0; j < 4; j++)
        acc[m][j] = mfma(__builtin_bit_cast(bf16x8, f.wv[j]), __builtin_bit_cast(bf16x8, f.h[m]), acc[m][j]);
    SB();
  };
  FragC f[NBUF];
#pragma unroll
  for (int i = 0; i < NBUF - 1; i++) ld(f[i], i);
#pragma unroll
  for (int i = 0; i < NB; i++) {
    if (i + NBUF - 1 < NB) ld(f[(i + NBUF - 1) % NBUF], i + NBUF - 1);
    mm(f[i % NBUF]);
  }
}

__global__ __launch_bounds__(512, 2) void kc_out(const char* __restrict__ x, char* __restrict__ ws,
                                                 char* __restrict__ out) {
  extern __shared__ __attribute__((aligned(16))) char lds[];
  const int b = blockIdx.x, xcd = b & 7, i = b >> 3;
  const int unit = xcd * 30 + i;
  if (unit >= 234) return;
  const int tb = unit / 9, db = unit - tb * 9;
  const int l = threadIdx.x & 63, g = l >> 4, c16 = l & 15;
  const int w = __builtin_amdgcn_readfirstlane(threadIdx.x >> 6);
  char* ws_dbg = ws; TS(3, 0); TS(3, 1);
  f32x4 acc[5][4];
#pragma unroll
  for (int m = 0; m < 5; m++)
#pragma unroll
    for (int j = 0; j < 4; j++) acc[m][j] = (f32x4){0.f, 0.f, 0.f, 0.f};
  unsigned ho[5];
  if (w < 6) {
#pragma unroll
    for (int m = 0; m < 5; m++) ho[m] = min(5 * tb + m, 127) * 73728 + l * 16;
    kc_stream<12, 6>(ws + WS_H + w * 1024, ho, ws + WS_WF + (size_t)(w * 36 + db * 4) * 1024, l * 16, acc);
  } else {
#pragma unroll
    for (int m = 0; m < 5; m++) ho[m] = min(5 * tb + m, 127) * 18432 + l * 16;
    kc_stream<9, 2>(ws + WS_O + (w - 6) * 1024, ho, ws + WS_WF + (size_t)((72 + w - 6) * 36 + db * 4) * 1024, l * 16, acc);
  }
  TS(3, 2); TW(3, 0);
  u32x2 xv[3];
#pragma unroll
  for (int q = 0; q < 3; q++) {
    const int st = min(w + 8 * q, 19), ts = min(5 * tb + (st >> 2), 127);
    xv[q] = *(const u32x2*)(x + (size_t)(ts * 16 + c16) * 1152 + (db * 64 + (st & 3) * 16 + 4 * g) * 2);
  }
  f32x4* red = (f32x4*)lds;
#pragma unroll
  for (int m = 0; m < 5; m++)
#pragma unroll
    for (int j = 0; j < 4; j++) red[(w * 20 + m * 4 + j) * 64 + l] = acc[m][j];
  asm volatile("s_waitcnt lgkmcnt(0)" ::: "memory");
  TW(3, 1);
  lds_bar();
  TS(3, 3);
#pragma unroll
  for (int q = 0; q < 3; q++) {
    const int st = w + 8 * q, ts = 5 * tb + (st >> 2);
    if (st < 20 && ts < 128) {
      const f32x4* p = red + st * 64 + l;
      f32x4 y = (p[0] + p[20 * 64]) + (p[40 * 64] + p[60 * 64]) + (p[80 * 64] + p[100 * 64]);
      f32x4 sv = p[120 * 64] + p[140 * 64];
      const float xf[4] = {bflo(xv[q][0]), bfhi(xv[q][0]), bflo(xv[q][1]), bfhi(xv[q][1])};
      float o[4];
#pragma unroll
      for (int e = 0; e < 4; e++) {
        const float sig = __builtin_amdgcn_rcpf(1.0f + __builtin_amdgcn_exp2f(-1.4426950408889634f * y[e]));
        o[e] = xf[e] + sv[e] * (y[e] * sig);
      }
      *(u32x2*)(out + (size_t)(ts * 16 + c16) * 1152 + (db * 64 + (st & 3) * 16 + 4 * g) * 2) =
          (u32x2){pk2(o[0], o[1]), pk2(o[2], o[3])};
    }
  }
  TS(3, 4);
}

extern "C" void kernel_launch(void* const* d_in, const int* in_sizes, int n_in, void* d_out, int out_size,
                              void* d_ws, size_t ws_size, hipStream_t stream) {
  const char* x = (const char*)d_in[0];
  const char* wqkv = (const char*)d_in[1];
  const char* wo = (const char*)d_in[2];
  const char* w_in = (const char*)d_in[4];
  const char* w_sp = (const char*)d_in[5];
  const char* b_sp = (const char*)d_in[6];
  const char* w_out = (const char*)d_in[7];
  char* ws = (char*)d_ws;
  (void)hipFuncSetAttribute((const void*)k1_proj, hipFuncAttributeMaxDynamicSharedMemorySize, K1_LDS);
  (void)hipFuncSetAttribute((const void*)k2_sgu, hipFuncAttributeMaxDynamicSharedMemorySize, G_LDS);
  (void)hipFuncSetAttribute((const void*)kc_out, hipFuncAttributeMaxDynamicSharedMemorySize, KC_LDS);
  k0_ln<<<256 + 810, 64, 0, stream>>>(x, wo, w_out, ws);
  k1_proj<<<256, 512, K1_LDS, stream>>>(w_in, wqkv, ws);
  k2_sgu<<<256, 512, G_LDS, stream>>>(w_sp, b_sp, ws);
  kc_out<<<8 * 30, 512, KC_LDS, stream>>>(x, ws, (char*)d_out);
}
```

```cpp
#define PF_K0_W
#define PF_K1_SP
#include <hip/hip_runtime.h>
#include <hip/hip_bf16.h>
#include <stdint.h>
#pragma clang diagnostic ignored "-Winline-asm"

typedef __attribute__((ext_vector_type(8))) __bf16 bf16x8;
typedef __attribute__((ext_vector_type(4))) __bf16 bf16x4;
typedef __attribute__((ext_vector_type(2))) __bf16 bf16x2;
typedef __attribute__((ext_vector_type(4))) float f32x4;
typedef __attribute__((ext_vector_type(2))) float f32x2;
typedef __attribute__((ext_vector_type(4))) unsigned u32x4;
typedef __attribute__((ext_vector_type(2))) unsigned u32x2;
typedef u32x4 u32x4_a4 __attribute__((aligned(4)));
typedef u32x2 u32x2_a4 __attribute__((aligned(4)));
#define AS1 __attribute__((address_space(1)))
#define AS3 __attribute__((address_space(3)))
#define DEVI __device__ __forceinline__

constexpr int S_ = 2048, D_ = 576, FH = 2304;

constexpr size_t WS_XN   = 0;
constexpr size_t WS_Z1   = WS_XN + (size_t)S_ * D_ * 2;
constexpr size_t WS_Z2   = WS_Z1 + (size_t)S_ * FH * 2;
constexpr size_t WS_H    = WS_Z2 + (size_t)S_ * FH * 2;
constexpr size_t WS_QKV  = WS_H + (size_t)S_ * FH * 2;
constexpr size_t WS_O    = WS_QKV + (size_t)96 * S_ * 18 * 2;
constexpr size_t WS_PART = WS_O + (size_t)S_ * D_ * 2;
constexpr size_t WS_STAT = WS_PART + (size_t)4 * S_ * D_ * 4;
constexpr size_t WS_WF   = WS_STAT + (size_t)16 * 16 * 128 * 8;

DEVI void glds16(const char* g, char* lds) {
  __builtin_amdgcn_global_load_lds((const AS1 void*)g, (AS3 void*)lds, 16, 0, 0);
}
DEVI void glds_s(unsigned voff, const char* sbase, unsigned ldsa) {
  asm volatile("s_mov_b32 m0, %2\n\ts_nop 0\n\tglobal_load_lds_dwordx4 %0, %1" ::"v"(voff), "s"(sbase), "s"(ldsa) : "memory", "m0");
}
DEVI void glds_s16(unsigned voff, const char* sbase, unsigned ldsa) {
  asm volatile("s_mov_b32 m0, %2\n\ts_mov_b64 exec, 0xffff\n\tglobal_load_lds_dwordx4 %0, %1\n\ts_mov_b64 exec, -1"
               ::"v"(voff), "s"(sbase), "s"(ldsa) : "memory", "m0");
}
DEVI void glds_s8(unsigned voff, const char* sbase, unsigned ldsa) {
  asm volatile("s_mov_b32 m0, %2\n\ts_mov_b64 exec, 0xff\n\tglobal_load_lds_dwordx4 %0, %1\n\ts_mov_b64 exec, -1"
               ::"v"(voff), "s"(sbase), "s"(ldsa) : "memory", "m0");
}
DEVI void touch_s(unsigned voff, const char* sbase, unsigned ldsa) {
  asm volatile("s_mov_b32 m0, %2\n\ts_nop 0\n\tglobal_load_lds_dword %0, %1" ::"v"(voff), "s"(sbase), "s"(ldsa) : "memory", "m0");
}
#define SB() __builtin_amdgcn_sched_barrier(0)
#define TS(K, I)
#define TW(K, I)
DEVI bf16x8 lds_b128(const char* p) { return *(const AS3 bf16x8*)p; }
DEVI bf16x4 lds_tr(const char* p) { return __builtin_amdgcn_ds_read_tr16_b64_v4bf16((AS3 bf16x4*)p); }
DEVI bf16x8 cat8(bf16x4 a, bf16x4 b) { return __builtin_shufflevector(a, b, 0, 1, 2, 3, 4, 5, 6, 7); }
DEVI f32x4 mfma(bf16x8 a, bf16x8 b, f32x4 c) { return __builtin_amdgcn_mfma_f32_16x16x32_bf16(a, b, c, 0, 0, 0); }
DEVI unsigned pk2(float a, float b) {
  f32x2 v = {a, b};
  bf16x2 r = __builtin_convertvector(v, bf16x2);
  return __builtin_bit_cast(unsigned, r);
}
DEVI float bflo(unsigned u) { return __builtin_bit_cast(float, u << 16); }
DEVI float bfhi(unsigned u) { return __builtin_bit_cast(float, u & 0xffff0000u); }
template <int N> DEVI void wait_vm_lgkm0() { asm volatile("s_waitcnt vmcnt(%0) lgkmcnt(0)" ::"n"(N) : "memory"); }
DEVI void bar() { __builtin_amdgcn_s_barrier(); asm volatile("" ::: "memory"); }
DEVI void lds_bar() { asm volatile("s_waitcnt lgkmcnt(0)" ::: "memory"); bar(); }
DEVI float gelu_t(float y) {
  float p = __builtin_fmaf(y * y, -0.1029432397f, -2.3022081983f);
  float e = __builtin_amdgcn_exp2f(y * p);
  return y * __builtin_amdgcn_rcpf(1.0f + e);
}
DEVI float fmx(float a, float b) { return __builtin_elementwise_maximum(a, b); }
DEVI int swap23(int r) { return (r & ~12) | ((r & 4) << 1) | ((r & 8) >> 1); }

DEVI void wprep(const char* __restrict__ wo, const char* __restrict__ w_out, char* __restrict__ ws, int kb, int dq, int l) {
  const int g = l >> 4, c16 = l & 15;
  unsigned short v[4][8];
#pragma unroll
  for (int e = 0; e < 8; e++) {
    const int r = e & 3, J = 2 * (kb < 72 ? kb : kb - 72) + (e >> 2);
    const char* src;
    if (kb < 72) src = w_out + (size_t)(16 * J + 4 * g + r) * 1152;
    else if (J < 32) src = wo + (size_t)(18 * J + 4 * g + r) * 1152;
    else src = wo + (size_t)((8 * (J - 32) + 2 * g + (r >> 1)) * 18 + 16 + (r & 1)) * 1152;
#pragma unroll
    for (int j = 0; j < 4; j++) v[j][e] = *(const unsigned short*)(src + (dq * 64 + j * 16 + c16) * 2);
  }
#pragma unroll
  for (int j = 0; j < 4; j++) {
    u32x4 o;
#pragma unroll
    for (int e = 0; e < 4; e++) o[e] = (unsigned)v[j][2 * e] | ((unsigned)v[j][2 * e + 1] << 16);
    *(u32x4*)(ws + WS_WF + ((size_t)kb * 36 + dq * 4 + j) * 1024 + l * 16) = o;
  }
}

__global__ __launch_bounds__(64) void k0_ln(const char* __restrict__ x, const char* __restrict__ wo,
                                            const char* __restrict__ w_out, char* __restrict__ ws,
                                            const char* __restrict__ w_in, const char* __restrict__ wqkv,
                                            const char* __restrict__ w_sp) {
  __shared__ __attribute__((aligned(16))) char pf[256];
  const int l = threadIdx.x, b = blockIdx.x;
  if (b >= 256 + 810) {
    const int u = b - 256 - 810;
    const unsigned lb = (unsigned)(uintptr_t)(AS3 char*)pf;
#pragma unroll
    for (int i = 0; i < 4; i++) {
      const int ln = (u * 4 + i) * 64;
      if (ln < 41472) touch_s(l * 128, w_in + (size_t)ln * 128, lb);
      else if (ln < 41472 + 15552) touch_s(l * 128, wqkv + (size_t)(ln - 41472) * 128, lb);
    }
    asm volatile("s_waitcnt vmcnt(0)" ::: "memory");
    return;
  }
  if (b >= 256) { wprep(wo, w_out, ws, (b - 256) / 9, (b - 256) % 9, l); return; }
  const int row = b * 8 + (l >> 3), c0 = l & 7;
  char* ws_dbg = ws; TS(0, 0);
  const char* src = x + (size_t)row * 1152 + c0 * 16;
  u32x4 v[9];
#pragma unroll
  for (int i = 0; i < 9; i++) v[i] = *(const u32x4*)(src + i * 128);
  *(u32x4*)(ws + WS_STAT + (size_t)b * 1024 + l * 16) = (u32x4){~0u, ~0u, ~0u, ~0u};
  float s = 0.f;
#pragma unroll
  for (int i = 0; i < 9; i++)
#pragma unroll
    for (int e = 0; e < 4; e++) s += bflo(v[i][e]) + bfhi(v[i][e]);
  s += __shfl_xor(s, 1); s += __shfl_xor(s, 2); s += __shfl_xor(s, 4);
  const float mean = s * (1.0f / 576.0f);
  float q = 0.f;
#pragma unroll
  for (int i = 0; i < 9; i++)
#pragma unroll
    for (int e = 0; e < 4; e++) {
      float a = bflo(v[i][e]) - mean, c = bfhi(v[i][e]) - mean;
      q += a * a + c * c;
    }
  q += __shfl_xor(q, 1); q += __shfl_xor(q, 2); q += __shfl_xor(q, 4);
  const float rs = 1.0f / __builtin_sqrtf(q * (1.0f / 576.0f) + 1e-5f);
  const int r = row & 127;
  char* dst = ws + WS_XN + (size_t)((row >> 7) * 18 + (c0 >> 2)) * 8192 + r * 64 + (((c0 & 3) ^ ((-(r >> 2)) & 3)) * 16);
#pragma unroll
  for (int i = 0; i < 9; i++) {
    u32x4 o;
#pragma unroll
    for (int e = 0; e < 4; e++) o[e] = pk2((bflo(v[i][e]) - mean) * rs, (bfhi(v[i][e]) - mean) * rs);
    *(u32x4*)(dst + i * 16384) = o;
  }
  TS(0, 4);
}

constexpr int K1_SLOT = 8192 + 25600;
constexpr int K1_STAT = 4 * K1_SLOT;
constexpr int K1_STAT2 = K1_STAT + 4096;
constexpr int K1_PF = K1_STAT2 + 4096;
constexpr int K1_LDS = K1_PF + 1024;
constexpr int K1_QIMG = 0;
constexpr int K1_ZIMG = 32768;

template <int NJ> struct Frag1 { bf16x8 a[NJ]; bf16x8 b[4]; };

struct K1Stage {
  unsigned xo, wo[4], xdst, wdst;
  const char* Xb;
  const char* Wa;
  const char* Wc;
  const char* Wq;
  unsigned cstep;
  DEVI void init(char* lds, const char* w_in, const char* wqkv, const char* ws, int rb, int cg, int w) {
    const int l = threadIdx.x & 63;
    const int qstart = 108 * cg - ((cg & 1) ? 4 : 0);
    Xb = ws + WS_XN + (size_t)rb * 18 * 8192;
    xo = w * 1024 + l * 16;
#pragma unroll
    for (int i = 0; i < 4; i++) {
      const int ins = (i < 3) ? w + 8 * i : 24;
      if (ins < 18) {
        const int ty = ins >= 9, q = (ins - 9 * ty) * 64 + l;
        const int rho = q / 18, c = q - rho * 18;
        wo[i] = swap23(rho) * 9216 + ((ty ? 0 : FH) + 144 * cg + c * 8) * 2;
      } else {
        const int q = (i < 3) ? (ins - 18) * 64 + l : 6 * 64 + w * 8 + (l & 7);
        const int rho = q / 14, c = q - rho * 14;
        wo[i] = swap23(rho) * 3456 + (qstart + c * 8) * 2;
      }
    }
    Wa = w_in; Wq = wqkv;
    Wc = (w < 2) ? w_in : wqkv;
    cstep = (w < 2) ? 32 * 9216 : 32 * 3456;
    const unsigned lb = (unsigned)(uintptr_t)(AS3 char*)lds;
    xdst = lb + w * 1024;
    wdst = lb + 8192 + w * 1024;
  }
  DEVI void piece(int t, int i, int w) const {
    const unsigned so = (t & 3) * K1_SLOT;
    if (i == 0) glds_s(xo, Xb + t * 8192, xdst + so);
    else if (i == 1) glds_s(wo[0], Wa + (size_t)t * (32 * 9216), wdst + so);
    else if (i == 2) glds_s(wo[1], Wa + (size_t)t * (32 * 9216), wdst + so + 8192);
    else if (i == 3) glds_s(wo[2], Wc + (size_t)t * cstep, wdst + so + 16384);
    else glds_s8(wo[3], Wq + (size_t)t * (32 * 3456), wdst + so + 24576 - w * 896);
  }
  DEVI void issue(int t, int w) const {
#pragma unroll
    for (int i = 0; i < 5; i++) piece(t, i, w);
  }
};

DEVI f32x2 gelu2(f32x2 y) {
  const f32x2 c1 = {-0.1029432397f, -0.1029432397f}, c0 = {-2.3022081983f, -2.3022081983f};
  const f32x2 a = y * __builtin_elementwise_fma(y * y, c1, c0);
  const float a0 = a[0], a1 = a[1];
  const f32x2 d = (f32x2){__builtin_amdgcn_exp2f(a0), __builtin_amdgcn_exp2f(a1)} + (f32x2){1.0f, 1.0f};
  const float d0 = d[0], d1 = d[1];
  return y * (f32x2){__builtin_amdgcn_rcpf(d0), __builtin_amdgcn_rcpf(d1)};
}
DEVI unsigned pk2v(f32x2 v) { return __builtin_bit_cast(unsigned, __builtin_convertvector(v, bf16x2)); }
DEVI f32x2 lo2(f32x4 v) { return __builtin_shufflevector(v, v, 0, 1); }
DEVI f32x2 hi2(f32x4 v) { return __builtin_shufflevector(v, v, 2, 3); }

template <int NQ>
DEVI void k1_body(char* lds, const K1Stage& gs, char* __restrict__ ws, const char* __restrict__ w_sp,
                  int rb, int cg, int w, int sh) {
  constexpr int NJ = (NQ == 0) ? 7 : 6;
  constexpr int NZ2 = (NQ == 0) ? 3 : 2;
  constexpr int NZ1 = (NQ == 1) ? 3 : 2;
  constexpr int Z2I = (NQ == 0) ? 0 : (NQ == 1) ? 3 : (NQ == 2) ? 5 : 7;
  constexpr int Z1I = (NQ == 0) ? 0 : (NQ == 1) ? 2 : (NQ == 2) ? 5 : 7;
  constexpr int QI  = (NQ == 0) ? 0 : (NQ == 1) ? 2 : (NQ == 2) ? 3 : 5;
  constexpr int NS = 18;
  char* ws_dbg = ws;
  const int tid = threadIdx.x, l = tid & 63, g = l >> 4, c16 = l & 15;
  const int qstart = 108 * cg - ((cg & 1) ? 4 : 0);

  const int boff = (64 * sh + c16) * 64 + ((g ^ ((-(c16 >> 2)) & 3)) * 16);
  const int rowp = 16 * (g >> 1) + 4 * (g & 1) + (c16 >> 2);
  const int aoffz = 8192 + rowp * 288 + 8 * (c16 & 3);
  const int aoffq = 8192 + 18432 + rowp * 224 + 8 * (c16 & 3);
  auto rda = [&](const char* slot, int j, int h) -> bf16x4 {
    if (j < NZ2) return lds_tr(slot + aoffz + (Z2I + j) * 32 + h * 8 * 288);
    if (j < NZ2 + NZ1) return lds_tr(slot + aoffz + 9216 + (Z1I + j - NZ2) * 32 + h * 8 * 288);
    return lds_tr(slot + aoffq + (QI + j - NZ2 - NZ1) * 32 + h * 8 * 224);
  };
  auto rd = [&](Frag1<NJ>& f, int t) {
    const char* slot = lds + (t & 3) * K1_SLOT;
#pragma unroll
    for (int m = 0; m < 4; m++) f.b[m] = lds_b128(slot + boff + m * 1024);
#pragma unroll
    for (int j = 0; j < NJ; j++) f.a[j] = cat8(rda(slot, j, 0), rda(slot, j, 1));
  };
  f32x4 acc[NJ][4];
#pragma unroll
  for (int j = 0; j < NJ; j++)
#pragma unroll
    for (int m = 0; m < 4; m++) acc[j][m] = (f32x4){0.f, 0.f, 0.f, 0.f};
  auto step = [&](const Frag1<NJ>& cur, Frag1<NJ>& nxt, int t, bool doissue) {
    const char* slot = lds + ((t + 1) & 3) * K1_SLOT;
    bf16x4 lo[NJ];
#pragma unroll
    for (int x = 0; x < 4 * NJ; x++) {
      const int j = x >> 2, m = x & 3;
      acc[j][m] = mfma(cur.a[j], cur.b[m], acc[j][m]);
      if (x < 4) nxt.b[x] = lds_b128(slot + boff + x * 1024);
      else if (x < 4 + 2 * NJ) {
        const int y = x - 4, jj = y >> 1;
        if ((y & 1) == 0) lo[jj] = rda(slot, jj, 0);
        else nxt.a[jj] = cat8(lo[jj], rda(slot, jj, 1));
      }
      if (doissue && (x & 3) == 1 && (x >> 2) < 5) gs.piece(t + 4, x >> 2, w);
      SB();
    }
  };
  Frag1<NJ> f0, f1;
  wait_vm_lgkm0<15>();
  bar();
  TS(1, 1);
  rd(f0, 0);
#pragma unroll 1
  for (int t = 0; t < NS - 4; t += 2) {
    wait_vm_lgkm0<10>(); bar(); SB();
    step(f0, f1, t, true);
    wait_vm_lgkm0<10>(); bar(); SB();
    step(f1, f0, t + 1, true);
  }
  wait_vm_lgkm0<10>(); bar(); SB(); step(f0, f1, NS - 4, false);
  wait_vm_lgkm0<5>();  bar(); SB(); step(f1, f0, NS - 3, false);
  wait_vm_lgkm0<0>();  bar(); SB(); step(f0, f1, NS - 2, false);
#pragma unroll
  for (int j = 0; j < NJ; j++)
#pragma unroll
    for (int m = 0; m < 4; m++) acc[j][m] = mfma(f1.a[j], f1.b[m], acc[j][m]);
  TS(1, 2);
  if (w < 4) touch_s((w * 64 + l) * 128, w_sp + (size_t)blockIdx.x * 32768, (unsigned)(uintptr_t)(AS3 char*)lds + K1_PF + w * 256);
  {
    f32x2* st = (f32x2*)(lds + K1_STAT) + NQ * 128 + 64 * sh + c16;
#pragma unroll
    for (int m = 0; m < 4; m++) {
      f32x2 a2 = {0.f, 0.f}, q2 = a2;
#pragma unroll
      for (int j = 0; j < NZ2; j++) {
        const f32x2 u = gelu2(lo2(acc[j][m])), v = gelu2(hi2(acc[j][m]));
        acc[j][m] = __builtin_shufflevector(u, v, 0, 1, 2, 3);
        a2 += u + v;
        q2 = __builtin_elementwise_fma(u, u, __builtin_elementwise_fma(v, v, q2));
      }
      float a = a2[0] + a2[1], q = q2[0] + q2[1];
      a += __shfl_xor(a, 16); q += __shfl_xor(q, 16);
      a += __shfl_xor(a, 32); q += __shfl_xor(q, 32);
      if (g == 0) st[16 * m] = (f32x2){a, q};
    }
  }
  lds_bar();
  AS1 unsigned long long* gstat = (AS1 unsigned long long*)(ws + WS_STAT) + (size_t)rb * 16 * 128;
  if (tid < 128) {
    const f32x2* st = (const f32x2*)(lds + K1_STAT) + tid;
    f32x2 v = st[0] + st[128] + st[256] + st[384];
    const float v0 = v[0], v1 = v[1];
    unsigned lo = __builtin_bit_cast(unsigned, v0), hi = __builtin_bit_cast(unsigned, v1);
    if (hi == 0xffffffffu) hi = 0x7fc00000u;
    __hip_atomic_store(gstat + cg * 128 + tid, ((unsigned long long)hi << 32) | lo, __ATOMIC_RELAXED,
                       __HIP_MEMORY_SCOPE_AGENT);
  }
#pragma unroll
  for (int j = NZ2 + NZ1; j < NJ; j++) {
#pragma unroll
    for (int p = 0; p < 2; p++) {
      const int c = (QI + (j - NZ2 - NZ1)) * 16 + 4 * g + 2 * p + qstart - 108 * cg;
      const int hp = (c * 3641) >> 16, d = c - hp * 18;
      char* dst = lds + K1_QIMG + hp * 4608 + (64 * sh + c16) * 36 + d * 2;
      if (c >= 0 && c < 108) {
#pragma unroll
        for (int m = 0; m < 4; m++) *(unsigned*)(dst + m * 16 * 36) = pk2(acc[j][m][2 * p], acc[j][m][2 * p + 1]);
      }
    }
  }
#pragma unroll
  for (int j = 0; j < NZ1; j++)
#pragma unroll
    for (int mp = 0; mp < 2; mp++) {
      const f32x4 u = acc[NZ2 + j][2 * mp], v = acc[NZ2 + j][2 * mp + 1];
      const u32x4 o = {pk2v(gelu2(lo2(u))), pk2v(gelu2(hi2(u))), pk2v(gelu2(lo2(v))), pk2v(gelu2(hi2(v)))};
      *(u32x4*)(ws + WS_Z1 + ((size_t)(4 * rb + 2 * sh + mp) * 144 + 9 * cg + Z1I + j) * 1024 + l * 16) = o;
    }
  const int prow = tid & 127, part = tid >> 7;
  unsigned long long sv[4];
#pragma unroll
  for (int i = 0; i < 4; i++)
    sv[i] = __hip_atomic_load(gstat + (part * 4 + i) * 128 + prow, __ATOMIC_RELAXED, __HIP_MEMORY_SCOPE_AGENT);
  lds_bar();
  TS(1, 3);
#pragma unroll
  for (int u = 0; u < 4; u++) {
    const int q = tid + 512 * u;
    if (u < 3 || q < 1728) {
      const int pi = (q * 3641) >> 20, rem = q - pi * 288;
      *(u32x4*)(ws + WS_QKV + ((size_t)(6 * cg + pi) * S_ + rb * 128) * 36 + rem * 16) =
          *(const u32x4*)(lds + K1_QIMG + q * 16);
    }
  }
  {
    float a = 0.f, q = 0.f;
#pragma unroll
    for (int i = 0; i < 4; i++) {
      unsigned long long v = sv[i];
      while ((unsigned)(v >> 32) == 0xffffffffu)
        v = __hip_atomic_load(gstat + (part * 4 + i) * 128 + prow, __ATOMIC_RELAXED, __HIP_MEMORY_SCOPE_AGENT);
      a += __builtin_bit_cast(float, (unsigned)v);
      q += __builtin_bit_cast(float, (unsigned)(v >> 32));
    }
    ((f32x2*)(lds + K1_STAT2))[part * 128 + prow] = (f32x2){a, q};
  }
  lds_bar();
#pragma unroll
  for (int m = 0; m < 4; m++) {
    const f32x2* st = (const f32x2*)(lds + K1_STAT2) + 64 * sh + 16 * m + c16;
    f32x2 v = st[0] + st[128] + st[256] + st[384];
    const float mean = v[0] * (1.0f / FH);
    const float var = fmaxf(v[1] * (1.0f / FH) - mean * mean, 0.f);
    const float rs = 1.0f / __builtin_sqrtf(var + 1e-5f);
    const f32x2 mean2 = {mean, mean}, rs2 = {rs, rs};
    char* dst = lds + K1_ZIMG + (64 * sh + 16 * m + swap23(c16)) * 288 + Z2I * 32 + g * 8;
#pragma unroll
    for (int j = 0; j < NZ2; j++)
      *(u32x2*)(dst + j * 32) = (u32x2){pk2v((lo2(acc[j][m]) - mean2) * rs2), pk2v((hi2(acc[j][m]) - mean2) * rs2)};
  }
  lds_bar();
  {
    char* zt = ws + WS_Z2 + (size_t)cg * 589824 + (size_t)rb * 36864;
#pragma unroll
    for (int u = 0; u < 5; u++) {
      const int q = tid + 512 * u;
      if (u < 4 || q < 2304) *(u32x4*)(zt + q * 16) = *(const u32x4*)(lds + K1_ZIMG + q * 16);
    }
  }
  asm volatile("s_waitcnt vmcnt(0)" ::: "memory");
  TS(1, 4);
}

__global__ __launch_bounds__(512, 2) void k1_proj(const char* __restrict__ w_in, const char* __restrict__ wqkv,
                                                  char* __restrict__ ws, const char* __restrict__ w_sp) {
  extern __shared__ __attribute__((aligned(16))) char lds[];
  const int b = blockIdx.x, xcd = b & 7, i = b >> 3;
  const int rb = 8 * (xcd & 1) + (i & 7), cg = 4 * (xcd >> 1) + (i >> 3);
  const int w = __builtin_amdgcn_readfirstlane(threadIdx.x >> 6);
  const int sh = w >> 2, nq = sh ? 3 - (w & 3) : (w & 3);
  char* ws_dbg = ws; TS(1, 0);
  K1Stage gs;
  gs.init(lds, w_in, wqkv, ws, rb, cg, w);
  gs.issue(0, w); gs.issue(1, w); gs.issue(2, w); gs.issue(3, w);
  if (nq == 0) k1_body<0>(lds, gs, ws, w_sp, rb, cg, w, sh);
  else if (nq == 1) k1_body<1>(lds, gs, ws, w_sp, rb, cg, w, sh);
  else if (nq == 2) k1_body<2>(lds, gs, ws, w_sp, rb, cg, w, sh);
  else k1_body<3>(lds, gs, ws, w_sp, rb, cg, w, sh);
}

template <class Hook>
DEVI void attn_phase(char* lds, char* __restrict__ ws, Hook hook) {
  const int b = blockIdx.x, h = b & 31, qb = b >> 5;
  const int tid = threadIdx.x, l = tid & 63, g = l >> 4, c16 = l & 15;
  const int w = __builtin_amdgcn_readfirstlane(tid >> 6);
  const int q0 = qb * 256;
  char* ws_dbg = ws;
  const char* qkv = ws + WS_QKV;
  const int key = q0 - 256 + tid;
  u32x4 qv[2];
#pragma unroll
  for (int m = 0; m < 2; m++)
    qv[m] = *(const u32x4_a4*)(qkv + ((size_t)h * S_ + q0 + 32 * w + 16 * m + c16) * 36 + (g < 2 ? 16 * g : 20));
  const char* kp = qkv + ((size_t)(32 + h) * S_ + max(key, 0)) * 36;
  const char* vp = kp + (size_t)32 * S_ * 36;
  const u32x4 k0 = *(const u32x4_a4*)kp, k1 = *(const u32x4_a4*)(kp + 16);
  const unsigned k2 = *(const unsigned*)(kp + 32);
  const u32x4 v0 = *(const u32x4_a4*)vp, v1 = *(const u32x4_a4*)(vp + 16);
  const unsigned v2 = *(const unsigned*)(vp + 32);
  bf16x8 qf[2];
  {
    const int f = (-(tid >> 2)) & 3;
    char* kr = lds + tid * 64;
    *(u32x4*)(kr + ((0 ^ f) * 16)) = k0;
    *(u32x4*)(kr + ((1 ^ f) * 16)) = k1;
    *(u32x4*)(kr + ((2 ^ f) * 16)) = (u32x4){k2, key < 0 ? 0x0000f149u : 0u, 0, 0};
    *(u32x4*)(kr + ((3 ^ f) * 16)) = (u32x4){0, 0, 0, 0};
    char* vr = lds + 32768 + tid * 64;
    const int x = ((tid >> 2) & 1) * 32;
    *(u32x4*)(vr + (0 ^ x)) = v0;
    *(u32x4*)(vr + (16 ^ x)) = v1;
    *(u32x4*)(vr + (32 ^ x)) = (u32x4){v2, 0x00003f80u, 0, 0};
    *(u32x4*)(vr + (48 ^ x)) = (u32x4){0, 0, 0, 0};
  }
#pragma unroll
  for (int m = 0; m < 2; m++) {
    u32x4 v = qv[m];
    if (g >= 2) v = (u32x4){g == 2 ? v[3] : 0u, g == 2 ? 0x00003f80u : 0u, 0, 0};
    qf[m] = __builtin_bit_cast(bf16x8, v);
  }
  hook();
  lds_bar();
  TW(2, 0);
  const float sc = 0.23570226039551584f * 1.4426950408889634f;
  const int koff = (32 * w + c16) * 64 + ((g ^ ((-(c16 >> 2)) & 3)) * 16);
  const int voff = 32768 + (32 * w + 4 * g + (c16 >> 2)) * 64 + 8 * (c16 & 3);
  const int vx = (g & 1) * 32;
#pragma unroll
  for (int m = 0; m < 2; m++) {
    f32x4 s[17];
#pragma unroll
    for (int i = 0; i < 17; i++) {
      bf16x8 kf = lds_b128(lds + koff + (i + m) * 1024);
      s[i] = mfma(kf, qf[m], (f32x4){0.f, 0.f, 0.f, 0.f});
    }
    const float ninf = -__builtin_inff();
#pragma unroll
    for (int r = 0; r < 4; r++) {
      if (!(c16 < 4 * g + r)) s[0][r] = ninf;
      if (!(4 * g + r <= c16)) s[16][r] = ninf;
    }
    float mxa = ninf, mxb = ninf;
#pragma unroll
    for (int i = 0; i < 17; i++) { mxa = fmx(fmx(mxa, s[i][0]), s[i][1]); mxb = fmx(fmx(mxb, s[i][2]), s[i][3]); }
    float mx = fmx(mxa, mxb);
    mx = fmx(mx, __shfl_xor(mx, 16));
    mx = fmx(mx, __shfl_xor(mx, 32));
    const float nb = -mx * sc;
    const f32x2 sc2 = {sc, sc}, nb2 = {nb, nb};
    unsigned p[18][2];
#pragma unroll
    for (int i = 0; i < 17; i++) {
      const f32x2 ta = __builtin_elementwise_fma((f32x2){s[i][0], s[i][1]}, sc2, nb2);
      const f32x2 tb = __builtin_elementwise_fma((f32x2){s[i][2], s[i][3]}, sc2, nb2);
      const float a0 = ta[0], a1 = ta[1], b0 = tb[0], b1 = tb[1];
      p[i][0] = pk2(__builtin_amdgcn_exp2f(a0), __builtin_amdgcn_exp2f(a1));
      p[i][1] = pk2(__builtin_amdgcn_exp2f(b0), __builtin_amdgcn_exp2f(b1));
    }
    p[17][0] = 0; p[17][1] = 0;
    f32x4 o0 = {0.f, 0.f, 0.f, 0.f}, o1 = o0;
#pragma unroll
    for (int i = 0; i < 9; i++) {
      const int r0 = (2 * i + m) * 1024, r1 = (i < 8) ? r0 + 1024 : r0;
      bf16x8 pf = __builtin_bit_cast(bf16x8, (u32x4){p[2 * i][0], p[2 * i][1], p[2 * i + 1][0], p[2 * i + 1][1]});
      bf16x8 va = cat8(lds_tr(lds + voff + vx + r0), lds_tr(lds + voff + vx + r1));
      bf16x8 vb = cat8(lds_tr(lds + voff + (32 ^ vx) + r0), lds_tr(lds + voff + (32 ^ vx) + r1));
      o0 = mfma(va, pf, o0);
      o1 = mfma(vb, pf, o1);
    }
    const float den = __shfl(o1[2], c16);
    const float inv = 1.0f / den;
    char* op = ws + WS_O + (size_t)(16 * qb + 2 * w + m) * 18432;
    *(u32x2*)(op + (h >> 1) * 1024 + l * 16 + (h & 1) * 8) = (u32x2){pk2(o0[0] * inv, o0[1] * inv), pk2(o0[2] * inv, o0[3] * inv)};
    if (g == 0)
      *(unsigned*)(op + (16 + (h >> 4)) * 1024 + (((h & 7) >> 1) * 16 + c16) * 16 + ((h >> 3) & 1) * 8 + (h & 1) * 4) =
          pk2(o1[0] * inv, o1[1] * inv);
  }
  TW(2, 1);
}

constexpr int G_SLOT = 16384 + 18432;
constexpr int G_LDS = 4 * G_SLOT;
template <int NJ> struct Frag2 { bf16x8 a[NJ]; bf16x8 b[4]; };

struct GStage {
  unsigned bo[2], ao[3], bdst, adst;
  const char* Bb;
  const char* Ab;
  DEVI void init(char* lds, const char* B, const char* A, int w) {
    const int l = threadIdx.x & 63;
    Bb = B; Ab = A;
#pragma unroll
    for (int i = 0; i < 2; i++) {
      int r = (2 * w + i) * 8 + (l >> 3);
      int c = (l & 7) ^ ((r >> 1) & 7);
      bo[i] = r * 4096 + c * 16;
    }
#pragma unroll
    for (int i = 0; i < 3; i++) {
      int q = w * 144 + i * 64 + ((i == 2) ? (l & 15) : l);
      ao[i] = q * 16;
    }
    const unsigned lb = (unsigned)(uintptr_t)(AS3 char*)lds;
    bdst = lb + w * 2048; adst = lb + 16384 + w * 2304;
  }
  DEVI void piece(int t, int i) const {
    const unsigned so = (t & 3) * G_SLOT;
    const size_t astep = 18432;
    if (i < 2) glds_s(bo[i], Bb + t * 128, bdst + so + i * 1024);
    else if (i < 4) glds_s(ao[i - 2], Ab + (size_t)t * astep, adst + so + (i - 2) * 1024);
    else glds_s16(ao[2], Ab + (size_t)t * astep, adst + so + 2048);
  }
  DEVI void issue(int t) const {
#pragma unroll
    for (int i = 0; i < 5; i++) piece(t, i);
  }
};

template <int NJ, int NS>
DEVI void gemm_core(char* lds, const GStage& gs, int w, f32x4 (&acc)[NJ][4], char* ws_dbg, int kid) {
  const int l = threadIdx.x & 63, g = l >> 4, c16 = l & 15;
  const int fh = w >> 2, th = w & 1, kh = (w >> 1) & 1;
  const int boff = (64 * th + c16) * 128 + (((4 * kh + g) ^ (c16 >> 1)) * 16);
  const int aoff = 16384 + (32 * kh + 16 * (g >> 1) + 4 * (g & 1) + (c16 >> 2)) * 288 + fh * 160 + 8 * (c16 & 3);
  auto rd = [&](Frag2<NJ>& f, int t) {
    const char* slot = lds + (t & 3) * G_SLOT;
#pragma unroll
    for (int m = 0; m < 4; m++) f.b[m] = lds_b128(slot + boff + m * 2048);
#pragma unroll
    for (int j = 0; j < NJ; j++)
      f.a[j] = cat8(lds_tr(slot + aoff + j * 32), lds_tr(slot + aoff + j * 32 + 8 * 288));
  };
  auto step = [&](const Frag2<NJ>& cur, Frag2<NJ>& nxt, int t, bool doissue) {
    const char* slot = lds + ((t + 1) & 3) * G_SLOT;
    const char* pb = slot + boff;
    const char* pa = slot + aoff;
    bf16x4 lo[NJ];
#pragma unroll
    for (int x = 0; x < 4 * NJ; x++) {
      const int j = x >> 2, m = x & 3;
      acc[j][m] = mfma(cur.a[j], cur.b[m], acc[j][m]);
      if (x < 4) nxt.b[x] = lds_b128(pb + x * 2048);
      else if (x < 4 + 2 * NJ) {
        const int y = x - 4, jj = y >> 1;
        if ((y & 1) == 0) lo[jj] = lds_tr(pa + jj * 32);
        else nxt.a[jj] = cat8(lo[jj], lds_tr(pa + jj * 32 + 8 * 288));
      }
      if (doissue && (x % 3) == 1 && x / 3 < 5) gs.piece(t + 4, x / 3);
      SB();
    }
  };
  auto mm = [&](const Frag2<NJ>& f) {
#pragma unroll
    for (int j = 0; j < NJ; j++)
#pragma unroll
      for (int m = 0; m < 4; m++) acc[j][m] = mfma(f.a[j], f.b[m], acc[j][m]);
  };
#pragma unroll
  for (int j = 0; j < NJ; j++)
#pragma unroll
    for (int m = 0; m < 4; m++) acc[j][m] = (f32x4){0.f, 0.f, 0.f, 0.f};
  Frag2<NJ> f0, f1;
  wait_vm_lgkm0<15>();
  bar();
  TS(kid, 1);
  rd(f0, 0);
  constexpr int M = NS - 4, MP = M & ~1;
#pragma unroll 1
  for (int t = 0; t < MP; t += 2) {
    wait_vm_lgkm0<10>(); bar(); SB();
    step(f0, f1, t, true);
    wait_vm_lgkm0<10>(); bar(); SB();
    step(f1, f0, t + 1, true);
  }
  if constexpr (M & 1) {
    wait_vm_lgkm0<10>(); bar(); SB(); step(f0, f1, NS - 5, true);
    wait_vm_lgkm0<10>(); bar(); SB(); step(f1, f0, NS - 4, false);
    wait_vm_lgkm0<5>();  bar(); SB(); step(f0, f1, NS - 3, false);
    wait_vm_lgkm0<0>();  bar(); SB(); step(f1, f0, NS - 2, false);
    mm(f0);
  } else {
    wait_vm_lgkm0<10>(); bar(); SB(); step(f0, f1, NS - 4, false);
    wait_vm_lgkm0<5>();  bar(); SB(); step(f1, f0, NS - 3, false);
    wait_vm_lgkm0<0>();  bar(); SB(); step(f0, f1, NS - 2, false);
    mm(f1);
  }
  TS(kid, 2);
}

template <int NJ>
DEVI void khalf_exchange(char* lds, int w, f32x4 (&acc)[NJ][4]) {
  const int l = threadIdx.x & 63, kh = (w >> 1) & 1;
  lds_bar();
  f32x4* mine = (f32x4*)(lds + w * 10240) + l;
  const f32x4* other = (const f32x4*)(lds + (w ^ 2) * 10240) + l;
  if (kh == 0) {
#pragma unroll
    for (int j = 0; j < NJ; j++) { mine[(2 * j) * 64] = acc[j][2]; mine[(2 * j + 1) * 64] = acc[j][3]; }
  } else {
#pragma unroll
    for (int j = 0; j < NJ; j++) {
      mine[(2 * j) * 64] = acc[j][0]; mine[(2 * j + 1) * 64] = acc[j][1];
      acc[j][0] = acc[j][2]; acc[j][1] = acc[j][3];
    }
  }
  lds_bar();
#pragma unroll
  for (int j = 0; j < NJ; j++) { acc[j][0] += other[(2 * j) * 64]; acc[j][1] += other[(2 * j + 1) * 64]; }
}

template <int NJ>
DEVI void k2_body(char* lds, const GStage& gs, const char* __restrict__ b_sp, char* __restrict__ ws,
                  int tb, int fb, int w) {
  f32x4 acc[NJ][4];
  gemm_core<NJ, 32>(lds, gs, w, acc, ws, 2);
  const int l = threadIdx.x & 63, g = l >> 4, c16 = l & 15;
  const int fh = w >> 2, th = w & 1, kh = (w >> 1) & 1;
  const int trow = tb * 128 + 64 * th + 32 * kh + c16;
  u32x4 z[NJ];
  float bs[2];
#pragma unroll
  for (int m = 0; m < 2; m++)
    bs[m] = __builtin_bit_cast(float, (unsigned)(*(const unsigned short*)(b_sp + (trow + 16 * m) * 2)) << 16);
#pragma unroll
  for (int j = 0; j < NJ; j++)
    z[j] = *(const u32x4*)(ws + WS_Z1 + ((size_t)(4 * tb + 2 * th + kh) * 144 + 9 * fb + 5 * fh + j) * 1024 + l * 16);
  khalf_exchange<NJ>(lds, w, acc);
  { char* ws_dbg = ws; TS(2, 3); }
#pragma unroll
  for (int m = 0; m < 2; m++)
#pragma unroll
    for (int j = 0; j < NJ; j++) {
      f32x4 a = acc[j][m];
      const unsigned z0 = z[j][2 * m], z1 = z[j][2 * m + 1];
      u32x2 o = {pk2(bflo(z0) * (a[0] + bs[m]), bfhi(z0) * (a[1] + bs[m])),
                 pk2(bflo(z1) * (a[2] + bs[m]), bfhi(z1) * (a[3] + bs[m]))};
      const int J = 9 * fb + 5 * fh + j;
      *(u32x2*)(ws + WS_H + (size_t)(8 * tb + 4 * th + 2 * kh + m) * 73728 + (J >> 1) * 1024 + l * 16 + (J & 1) * 8) = o;
    }
  { char* ws_dbg = ws; TS(2, 4); }
}
__global__ __launch_bounds__(512, 2) void k2_sgu(const char* __restrict__ w_sp, const char* __restrict__ b_sp,
                                                 char* __restrict__ ws) {
  extern __shared__ __attribute__((aligned(16))) char lds[];
  const int b = blockIdx.x, xcd = b & 7, i = b >> 3;
  const int tb = 8 * (xcd & 1) + (i & 7), fb = 4 * (xcd >> 1) + (i >> 3);
  const int w = __builtin_amdgcn_readfirstlane(threadIdx.x >> 6);
  char* ws_dbg = ws; TS(2, 0);
  GStage gs;
  gs.init(lds, w_sp + (size_t)tb * 128 * 4096, ws + WS_Z2 + (size_t)fb * 589824, w);
  attn_phase(lds + 2 * G_SLOT, ws, [&]() { gs.issue(0); gs.issue(1); });
  lds_bar();
  gs.issue(2); gs.issue(3);
  if (w < 4) k2_body<5>(lds, gs, b_sp, ws, tb, fb, w);
  else k2_body<4>(lds, gs, b_sp, ws, tb, fb, w);
}

constexpr int KC_LDS = 8 * 20 * 1024;
struct FragC { u32x4 h[5]; u32x4 wv[4]; };

template <int NB, int KSTEP>
DEVI void kc_stream(const char* __restrict__ hb, const unsigned (&ho)[5], const char* __restrict__ wb, unsigned wofs,
                    f32x4 (&acc)[5][4]) {
  constexpr int NBUF = 3;
  auto ld = [&](FragC& f, int i) {
    const int ii = i;
#pragma unroll
    for (int m = 0; m < 5; m++) f.h[m] = *(const u32x4*)(hb + (size_t)ii * (KSTEP * 1024) + ho[m]);
#pragma unroll
    for (int j = 0; j < 4; j++) f.wv[j] = *(const u32x4*)(wb + (size_t)ii * (KSTEP * 36 * 1024) + wofs + j * 1024);
    SB();
  };
  auto mm = [&](const FragC& f) {
#pragma unroll
    for (int m = 0; m < 5; m++)
#pragma unroll
      for (int j = 0; j < 4; j++)
        acc[m][j] = mfma(__builtin_bit_cast(bf16x8, f.wv[j]), __builtin_bit_cast(bf16x8, f.h[m]), acc[m][j]);
    SB();
  };
  FragC f[NBUF];
#pragma unroll
  for (int i = 0; i < NBUF - 1; i++) ld(f[i], i);
#pragma unroll
  for (int i = 0; i < NB; i++) {
    if (i + NBUF - 1 < NB) ld(f[(i + NBUF - 1) % NBUF], i + NBUF - 1);
    mm(f[i % NBUF]);
  }
}

__global__ __launch_bounds__(512, 2) void kc_out(const char* __restrict__ x, char* __restrict__ ws,
                                                 char* __restrict__ out) {
  extern __shared__ __attribute__((aligned(16))) char lds[];
  const int b = blockIdx.x, xcd = b & 7, i = b >> 3;
  const int unit = xcd * 30 + i;
  if (unit >= 234) return;
  const int tb = unit / 9, db = unit - tb * 9;
  const int l = threadIdx.x & 63, g = l >> 4, c16 = l & 15;
  const int w = __builtin_amdgcn_readfirstlane(threadIdx.x >> 6);
  char* ws_dbg = ws; TS(3, 0); TS(3, 1);
  f32x4 acc[5][4];
#pragma unroll
  for (int m = 0; m < 5; m++)
#pragma unroll
    for (int j = 0; j < 4; j++) acc[m][j] = (f32x4){0.f, 0.f, 0.f, 0.f};
  unsigned ho[5];
  if (w < 6) {
#pragma unroll
    for (int m = 0; m < 5; m++) ho[m] = min(5 * tb + m, 127) * 73728 + l * 16;
    kc_stream<12, 6>(ws + WS_H + w * 1024, ho, ws + WS_WF + (size_t)(w * 36 + db * 4) * 1024, l * 16, acc);
  } else {
#pragma unroll
    for (int m = 0; m < 5; m++) ho[m] = min(5 * tb + m, 127) * 18432 + l * 16;
    kc_stream<9, 2>(ws + WS_O + (w - 6) * 1024, ho, ws + WS_WF + (size_t)((72 + w - 6) * 36 + db * 4) * 1024, l * 16, acc);
  }
  TS(3, 2); TW(3, 0);
  u32x2 xv[3];
#pragma unroll
  for (int q = 0; q < 3; q++) {
    const int st = min(w + 8 * q, 19), ts = min(5 * tb + (st >> 2), 127);
    xv[q] = *(const u32x2*)(x + (size_t)(ts * 16 + c16) * 1152 + (db * 64 + (st & 3) * 16 + 4 * g) * 2);
  }
  f32x4* red = (f32x4*)lds;
#pragma unroll
  for (int m = 0; m < 5; m++)
#pragma unroll
    for (int j = 0; j < 4; j++) red[(w * 20 + m * 4 + j) * 64 + l] = acc[m][j];
  asm volatile("s_waitcnt lgkmcnt(0)" ::: "memory");
  TW(3, 1);
  lds_bar();
  TS(3, 3);
#pragma unroll
  for (int q = 0; q < 3; q++) {
    const int st = w + 8 * q, ts = 5 * tb + (st >> 2);
    if (st < 20 && ts < 128) {
      const f32x4* p = red + st * 64 + l;
      f32x4 y = (p[0] + p[20 * 64]) + (p[40 * 64] + p[60 * 64]) + (p[80 * 64] + p[100 * 64]);
      f32x4 sv = p[120 * 64] + p[140 * 64];
      const float xf[4] = {bflo(xv[q][0]), bfhi(xv[q][0]), bflo(xv[q][1]), bfhi(xv[q][1])};
      float o[4];
#pragma unroll
      for (int e = 0; e < 4; e++) {
        const float sig = __builtin_amdgcn_rcpf(1.0f + __builtin_amdgcn_exp2f(-1.4426950408889634f * y[e]));
        o[e] = xf[e] + sv[e] * (y[e] * sig);
      }
      *(u32x2*)(out + (size_t)(ts * 16 + c16) * 1152 + (db * 64 + (st & 3) * 16 + 4 * g) * 2) =
          (u32x2){pk2(o[0], o[1]), pk2(o[2], o[3])};
    }
  }
  TS(3, 4);
}

extern "C" void kernel_launch(void* const* d_in, const int* in_sizes, int n_in, void* d_out, int out_size,
                              void* d_ws, size_t ws_size, hipStream_t stream) {
  const char* x = (const char*)d_in[0];
  const char* wqkv = (const char*)d_in[1];
  const char* wo = (const char*)d_in[2];
  const char* w_in = (const char*)d_in[4];
  const char* w_sp = (const char*)d_in[5];
  const char* b_sp = (const char*)d_in[6];
  const char* w_out = (const char*)d_in[7];
  char* ws = (char*)d_ws;
  (void)hipFuncSetAttribute((const void*)k1_proj, hipFuncAttributeMaxDynamicSharedMemorySize, K1_LDS);
  (void)hipFuncSetAttribute((const void*)k2_sgu, hipFuncAttributeMaxDynamicSharedMemorySize, G_LDS);
  (void)hipFuncSetAttribute((const void*)kc_out, hipFuncAttributeMaxDynamicSharedMemorySize, KC_LDS);
  constexpr int NPF = 57344 / 256;
  k0_ln<<<256 + 810 + NPF, 64, 0, stream>>>(x, wo, w_out, ws, w_in, wqkv, w_sp);
  k1_proj<<<256, 512, K1_LDS, stream>>>(w_in, wqkv, ws, w_sp);
  k2_sgu<<<256, 512, G_LDS, stream>>>(w_sp, b_sp, ws);
  kc_out<<<8 * 30, 512, KC_LDS, stream>>>(x, ws, (char*)d_out);
}
```

```cpp
#include <hip/hip_runtime.h>
#include <hip/hip_bf16.h>
#include <stdint.h>
#pragma clang diagnostic ignored "-Winline-asm"

typedef __attribute__((ext_vector_type(8))) __bf16 bf16x8;
typedef __attribute__((ext_vector_type(4))) __bf16 bf16x4;
typedef __attribute__((ext_vector_type(2))) __bf16 bf16x2;
typedef __attribute__((ext_vector_type(4))) float f32x4;
typedef __attribute__((ext_vector_type(2))) float f32x2;
typedef __attribute__((ext_vector_type(4))) unsigned u32x4;
typedef __attribute__((ext_vector_type(2))) unsigned u32x2;
typedef u32x4 u32x4_a4 __attribute__((aligned(4)));
typedef u32x2 u32x2_a4 __attribute__((aligned(4)));
#define AS1 __attribute__((address_space(1)))
#define AS3 __attribute__((address_space(3)))
#define DEVI __device__ __forceinline__

constexpr int S_ = 2048, D_ = 576, FH = 2304;

constexpr size_t WS_XN   = 0;
constexpr size_t WS_Z1   = WS_XN + (size_t)S_ * D_ * 2;
constexpr size_t WS_Z2   = WS_Z1 + (size_t)S_ * FH * 2;
constexpr size_t WS_H    = WS_Z2 + (size_t)S_ * FH * 2;
constexpr size_t WS_QKV  = WS_H + (size_t)S_ * FH * 2;
constexpr size_t WS_O    = WS_QKV + (size_t)96 * S_ * 18 * 2;
constexpr size_t WS_PART = WS_O + (size_t)S_ * D_ * 2;
constexpr size_t WS_STAT = WS_PART + (size_t)4 * S_ * D_ * 4;
constexpr size_t WS_WF   = WS_STAT + (size_t)16 * 16 * 128 * 8;

DEVI void glds16(const char* g, char* lds) {
  __builtin_amdgcn_global_load_lds((const AS1 void*)g, (AS3 void*)lds, 16, 0, 0);
}
DEVI void glds_s(unsigned voff, const char* sbase, unsigned ldsa) {
  asm volatile("s_mov_b32 m0, %2\n\ts_nop 0\n\tglobal_load_lds_dwordx4 %0, %1" ::"v"(voff), "s"(sbase), "s"(ldsa) : "memory", "m0");
}
DEVI void glds_s16(unsigned voff, const char* sbase, unsigned ldsa) {
  asm volatile("s_mov_b32 m0, %2\n\ts_mov_b64 exec, 0xffff\n\tglobal_load_lds_dwordx4 %0, %1\n\ts_mov_b64 exec, -1"
               ::"v"(voff), "s"(sbase), "s"(ldsa) : "memory", "m0");
}
DEVI void glds_s8(unsigned voff, const char* sbase, unsigned ldsa) {
  asm volatile("s_mov_b32 m0, %2\n\ts_mov_b64 exec, 0xff\n\tglobal_load_lds_dwordx4 %0, %1\n\ts_mov_b64 exec, -1"
               ::"v"(voff), "s"(sbase), "s"(ldsa) : "memory", "m0");
}
DEVI void touch_s(unsigned voff, const char* sbase, unsigned ldsa) {
  asm volatile("s_mov_b32 m0, %2\n\ts_nop 0\n\tglobal_load_lds_dword %0, %1" ::"v"(voff), "s"(sbase), "s"(ldsa) : "memory", "m0");
}
DEVI void st16(char* p, u32x4 v) {
  asm volatile("global_store_dwordx4 %0, %1, off sc1\n\ts_nop 1" ::"v"(p), "v"(v) : "memory");
}
#define SB() __builtin_amdgcn_sched_barrier(0)
#define TS(K, I)
#define TW(K, I)
DEVI bf16x8 lds_b128(const char* p) { return *(const AS3 bf16x8*)p; }
DEVI bf16x4 lds_tr(const char* p) { return __builtin_amdgcn_ds_read_tr16_b64_v4bf16((AS3 bf16x4*)p); }
DEVI bf16x8 cat8(bf16x4 a, bf16x4 b) { return __builtin_shufflevector(a, b, 0, 1, 2, 3, 4, 5, 6, 7); }
DEVI f32x4 mfma(bf16x8 a, bf16x8 b, f32x4 c) { return __builtin_amdgcn_mfma_f32_16x16x32_bf16(a, b, c, 0, 0, 0); }
DEVI unsigned pk2(float a, float b) {
  f32x2 v = {a, b};
  bf16x2 r = __builtin_convertvector(v, bf16x2);
  return __builtin_bit_cast(unsigned, r);
}
DEVI float bflo(unsigned u) { return __builtin_bit_cast(float, u << 16); }
DEVI float bfhi(unsigned u) { return __builtin_bit_cast(float, u & 0xffff0000u); }
template <int N> DEVI void wait_vm_lgkm0() { asm volatile("s_waitcnt vmcnt(%0) lgkmcnt(0)" ::"n"(N) : "memory"); }
DEVI void bar() { __builtin_amdgcn_s_barrier(); asm volatile("" ::: "memory"); }
DEVI void lds_bar() { asm volatile("s_waitcnt lgkmcnt(0)" ::: "memory"); bar(); }
DEVI float gelu_t(float y) {
  float p = __builtin_fmaf(y * y, -0.1029432397f, -2.3022081983f);
  float e = __builtin_amdgcn_exp2f(y * p);
  return y * __builtin_amdgcn_rcpf(1.0f + e);
}
DEVI float fmx(float a, float b) { return __builtin_elementwise_maximum(a, b); }
DEVI int swap23(int r) { return (r & ~12) | ((r & 4) << 1) | ((r & 8) >> 1); }

struct WPrep {
  unsigned short v[4][8];
  DEVI void load(const char* __restrict__ wo, const char* __restrict__ w_out, int kb, int dq, int l) {
    const int g = l >> 4, c16 = l & 15;
#pragma unroll
    for (int e = 0; e < 8; e++) {
      const int r = e & 3, J = 2 * (kb < 72 ? kb : kb - 72) + (e >> 2);
      const char* src;
      if (kb < 72) src = w_out + (size_t)(16 * J + 4 * g + r) * 1152;
      else if (J < 32) src = wo + (size_t)(18 * J + 4 * g + r) * 1152;
      else src = wo + (size_t)((8 * (J - 32) + 2 * g + (r >> 1)) * 18 + 16 + (r & 1)) * 1152;
#pragma unroll
      for (int j = 0; j < 4; j++) v[j][e] = *(const unsigned short*)(src + (dq * 64 + j * 16 + c16) * 2);
    }
  }
  DEVI void store(char* __restrict__ ws, int kb, int dq, int l) const {
#pragma unroll
    for (int j = 0; j < 4; j++) {
      u32x4 o;
#pragma unroll
      for (int e = 0; e < 4; e++) o[e] = (unsigned)v[j][2 * e] | ((unsigned)v[j][2 * e + 1] << 16);
      *(u32x4*)(ws + WS_WF + ((size_t)kb * 36 + dq * 4 + j) * 1024 + l * 16) = o;
    }
  }
};

__global__ __launch_bounds__(64) void k0_ln(const char* __restrict__ x, char* __restrict__ ws,
                                            const char* __restrict__ w_in, const char* __restrict__ wqkv) {
  __shared__ __attribute__((aligned(16))) char pf[256];
  const int l = threadIdx.x, b = blockIdx.x;
  if (b >= 256) {
    const int u = b - 256;
    const unsigned lb = (unsigned)(uintptr_t)(AS3 char*)pf;
#pragma unroll
    for (int i = 0; i < 4; i++) {
      const int ln = (u * 4 + i) * 64;
      if (ln < 41472) touch_s(l * 128, w_in + (size_t)ln * 128, lb);
      else if (ln < 41472 + 15552) touch_s(l * 128, wqkv + (size_t)(ln - 41472) * 128, lb);
    }
    asm volatile("s_waitcnt vmcnt(0)" ::: "memory");
    return;
  }
  const int row = b * 8 + (l >> 3), c0 = l & 7;
  char* ws_dbg = ws; TS(0, 0);
  const char* src = x + (size_t)row * 1152 + c0 * 16;
  u32x4 v[9];
#pragma unroll
  for (int i = 0; i < 9; i++) v[i] = *(const u32x4*)(src + i * 128);
  *(u32x4*)(ws + WS_STAT + (size_t)b * 1024 + l * 16) = (u32x4){~0u, ~0u, ~0u, ~0u};
  float s = 0.f;
#pragma unroll
  for (int i = 0; i < 9; i++)
#pragma unroll
    for (int e = 0; e < 4; e++) s += bflo(v[i][e]) + bfhi(v[i][e]);
  s += __shfl_xor(s, 1); s += __shfl_xor(s, 2); s += __shfl_xor(s, 4);
  const float mean = s * (1.0f / 576.0f);
  float q = 0.f;
#pragma unroll
  for (int i = 0; i < 9; i++)
#pragma unroll
    for (int e = 0; e < 4; e++) {
      float a = bflo(v[i][e]) - mean, c = bfhi(v[i][e]) - mean;
      q += a * a + c * c;
    }
  q += __shfl_xor(q, 1); q += __shfl_xor(q, 2); q += __shfl_xor(q, 4);
  const float rs = 1.0f / __builtin_sqrtf(q * (1.0f / 576.0f) + 1e-5f);
  const int r = row & 127;
  char* dst = ws + WS_XN + (size_t)((row >> 7) * 18 + (c0 >> 2)) * 8192 + r * 64 + (((c0 & 3) ^ ((-(r >> 2)) & 3)) * 16);
#pragma unroll
  for (int i = 0; i < 9; i++) {
    u32x4 o;
#pragma unroll
    for (int e = 0; e < 4; e++) o[e] = pk2((bflo(v[i][e]) - mean) * rs, (bfhi(v[i][e]) - mean) * rs);
    *(u32x4*)(dst + i * 16384) = o;
  }
  TS(0, 4);
}

constexpr int K1_SLOT = 8192 + 25600;
constexpr int K1_STAT = 4 * K1_SLOT;
constexpr int K1_STAT2 = K1_STAT + 4096;
constexpr int K1_PF = K1_STAT2 + 4096;
constexpr int K1_LDS = K1_PF + 1024;
constexpr int K1_QIMG = 0;
constexpr int K1_ZIMG = 32768;

template <int NJ> struct Frag1 { bf16x8 a[NJ]; bf16x8 b[4]; };

struct K1Stage {
  unsigned xo, wo[4], xdst, wdst;
  const char* Xb;
  const char* Wa;
  const char* Wc;
  const char* Wq;
  unsigned cstep;
  DEVI void init(char* lds, const char* w_in, const char* wqkv, const char* ws, int rb, int cg, int w) {
    const int l = threadIdx.x & 63;
    const int qstart = 108 * cg - ((cg & 1) ? 4 : 0);
    Xb = ws + WS_XN + (size_t)rb * 18 * 8192;
    xo = w * 1024 + l * 16;
#pragma unroll
    for (int i = 0; i < 4; i++) {
      const int ins = (i < 3) ? w + 8 * i : 24;
      if (ins < 18) {
        const int ty = ins >= 9, q = (ins - 9 * ty) * 64 + l;
        const int rho = q / 18, c = q - rho * 18;
        wo[i] = swap23(rho) * 9216 + ((ty ? 0 : FH) + 144 * cg + c * 8) * 2;
      } else {
        const int q = (i < 3) ? (ins - 18) * 64 + l : 6 * 64 + w * 8 + (l & 7);
        const int rho = q / 14, c = q - rho * 14;
        wo[i] = swap23(rho) * 3456 + (qstart + c * 8) * 2;
      }
    }
    Wa = w_in; Wq = wqkv;
    Wc = (w < 2) ? w_in : wqkv;
    cstep = (w < 2) ? 32 * 9216 : 32 * 3456;
    const unsigned lb = (unsigned)(uintptr_t)(AS3 char*)lds;
    xdst = lb + w * 1024;
    wdst = lb + 8192 + w * 1024;
  }
  DEVI void piece(int t, int i, int w) const {
    const unsigned so = (t & 3) * K1_SLOT;
    if (i == 0) glds_s(xo, Xb + t * 8192, xdst + so);
    else if (i == 1) glds_s(wo[0], Wa + (size_t)t * (32 * 9216), wdst + so);
    else if (i == 2) glds_s(wo[1], Wa + (size_t)t * (32 * 9216), wdst + so + 8192);
    else if (i == 3) glds_s(wo[2], Wc + (size_t)t * cstep, wdst + so + 16384);
    else glds_s8(wo[3], Wq + (size_t)t * (32 * 3456), wdst + so + 24576 - w * 896);
  }
  DEVI void issue(int t, int w) const {
#pragma unroll
    for (int i = 0; i < 5; i++) piece(t, i, w);
  }
};

DEVI f32x2 gelu2(f32x2 y) {
  const f32x2 c1 = {-0.1029432397f, -0.1029432397f}, c0 = {-2.3022081983f, -2.3022081983f};
  const f32x2 a = y * __builtin_elementwise_fma(y * y, c1, c0);
  const float a0 = a[0], a1 = a[1];
  const f32x2 d = (f32x2){__builtin_amdgcn_exp2f(a0), __builtin_amdgcn_exp2f(a1)} + (f32x2){1.0f, 1.0f};
  const float d0 = d[0], d1 = d[1];
  return y * (f32x2){__builtin_amdgcn_rcpf(d0), __builtin_amdgcn_rcpf(d1)};
}
DEVI unsigned pk2v(f32x2 v) { return __builtin_bit_cast(unsigned, __builtin_convertvector(v, bf16x2)); }
DEVI f32x2 lo2(f32x4 v) { return __builtin_shufflevector(v, v, 0, 1); }
DEVI f32x2 hi2(f32x4 v) { return __builtin_shufflevector(v, v, 2, 3); }

template <int NQ>
DEVI void k1_body(char* lds, const K1Stage& gs, char* __restrict__ ws, const char* __restrict__ w_sp,
                  int rb, int cg, int w, int sh) {
  constexpr int NJ = (NQ == 0) ? 7 : 6;
  constexpr int NZ2 = (NQ == 0) ? 3 : 2;
  constexpr int NZ1 = (NQ == 1) ? 3 : 2;
  constexpr int Z2I = (NQ == 0) ? 0 : (NQ == 1) ? 3 : (NQ == 2) ? 5 : 7;
  constexpr int Z1I = (NQ == 0) ? 0 : (NQ == 1) ? 2 : (NQ == 2) ? 5 : 7;
  constexpr int QI  = (NQ == 0) ? 0 : (NQ == 1) ? 2 : (NQ == 2) ? 3 : 5;
  constexpr int NS = 18;
  char* ws_dbg = ws;
  const int tid = threadIdx.x, l = tid & 63, g = l >> 4, c16 = l & 15;
  const int qstart = 108 * cg - ((cg & 1) ? 4 : 0);

  const int boff = (64 * sh + c16) * 64 + ((g ^ ((-(c16 >> 2)) & 3)) * 16);
  const int rowp = 16 * (g >> 1) + 4 * (g & 1) + (c16 >> 2);
  const int aoffz = 8192 + rowp * 288 + 8 * (c16 & 3);
  const int aoffq = 8192 + 18432 + rowp * 224 + 8 * (c16 & 3);
  auto rda = [&](const char* slot, int j, int h) -> bf16x4 {
    if (j < NZ2) return lds_tr(slot + aoffz + (Z2I + j) * 32 + h * 8 * 288);
    if (j < NZ2 + NZ1) return lds_tr(slot + aoffz + 9216 + (Z1I + j - NZ2) * 32 + h * 8 * 288);
    return lds_tr(slot + aoffq + (QI + j - NZ2 - NZ1) * 32 + h * 8 * 224);
  };
  auto rd = [&](Frag1<NJ>& f, int t) {
    const char* slot = lds + (t & 3) * K1_SLOT;
#pragma unroll
    for (int m = 0; m < 4; m++) f.b[m] = lds_b128(slot + boff + m * 1024);
#pragma unroll
    for (int j = 0; j < NJ; j++) f.a[j] = cat8(rda(slot, j, 0), rda(slot, j, 1));
  };
  f32x4 acc[NJ][4];
#pragma unroll
  for (int j = 0; j < NJ; j++)
#pragma unroll
    for (int m = 0; m < 4; m++) acc[j][m] = (f32x4){0.f, 0.f, 0.f, 0.f};
  auto step = [&](const Frag1<NJ>& cur, Frag1<NJ>& nxt, int t, bool doissue) {
    const char* slot = lds + ((t + 1) & 3) * K1_SLOT;
    bf16x4 lo[NJ];
#pragma unroll
    for (int x = 0; x < 4 * NJ; x++) {
      const int j = x >> 2, m = x & 3;
      acc[j][m] = mfma(cur.a[j], cur.b[m], acc[j][m]);
      if (x < 4) nxt.b[x] = lds_b128(slot + boff + x * 1024);
      else if (x < 4 + 2 * NJ) {
        const int y = x - 4, jj = y >> 1;
        if ((y & 1) == 0) lo[jj] = rda(slot, jj, 0);
        else nxt.a[jj] = cat8(lo[jj], rda(slot, jj, 1));
      }
      if (doissue && (x & 3) == 1 && (x >> 2) < 5) gs.piece(t + 4, x >> 2, w);
      SB();
    }
  };
  Frag1<NJ> f0, f1;
  wait_vm_lgkm0<15>();
  bar();
  TS(1, 1);
  rd(f0, 0);
#pragma unroll 1
  for (int t = 0; t < NS - 4; t += 2) {
    wait_vm_lgkm0<10>(); bar(); SB();
    step(f0, f1, t, true);
    wait_vm_lgkm0<10>(); bar(); SB();
    step(f1, f0, t + 1, true);
  }
  wait_vm_lgkm0<10>(); bar(); SB(); step(f0, f1, NS - 4, false);
  wait_vm_lgkm0<5>();  bar(); SB(); step(f1, f0, NS - 3, false);
  wait_vm_lgkm0<0>();  bar(); SB(); step(f0, f1, NS - 2, false);
#pragma unroll
  for (int j = 0; j < NJ; j++)
#pragma unroll
    for (int m = 0; m < 4; m++) acc[j][m] = mfma(f1.a[j], f1.b[m], acc[j][m]);
  TS(1, 2);
  if (w < 4) touch_s((w * 64 + l) * 128, w_sp + (size_t)blockIdx.x * 32768, (unsigned)(uintptr_t)(AS3 char*)lds + K1_PF + w * 256);
  {
    f32x2* st = (f32x2*)(lds + K1_STAT) + NQ * 128 + 64 * sh + c16;
#pragma unroll
    for (int m = 0; m < 4; m++) {
      f32x2 a2 = {0.f, 0.f}, q2 = a2;
#pragma unroll
      for (int j = 0; j < NZ2; j++) {
        const f32x2 u = gelu2(lo2(acc[j][m])), v = gelu2(hi2(acc[j][m]));
        acc[j][m] = __builtin_shufflevector(u, v, 0, 1, 2, 3);
        a2 += u + v;
        q2 = __builtin_elementwise_fma(u, u, __builtin_elementwise_fma(v, v, q2));
      }
      float a = a2[0] + a2[1], q = q2[0] + q2[1];
      a += __shfl_xor(a, 16); q += __shfl_xor(q, 16);
      a += __shfl_xor(a, 32); q += __shfl_xor(q, 32);
      if (g == 0) st[16 * m] = (f32x2){a, q};
    }
  }
  lds_bar();
  AS1 unsigned long long* gstat = (AS1 unsigned long long*)(ws + WS_STAT) + (size_t)rb * 16 * 128;
  if (tid < 128) {
    const f32x2* st = (const f32x2*)(lds + K1_STAT) + tid;
    f32x2 v = st[0] + st[128] + st[256] + st[384];
    const float v0 = v[0], v1 = v[1];
    unsigned lo = __builtin_bit_cast(unsigned, v0), hi = __builtin_bit_cast(unsigned, v1);
    if (hi == 0xffffffffu) hi = 0x7fc00000u;
    __hip_atomic_store(gstat + cg * 128 + tid, ((unsigned long long)hi << 32) | lo, __ATOMIC_RELAXED,
                       __HIP_MEMORY_SCOPE_AGENT);
  }
#pragma unroll
  for (int j = NZ2 + NZ1; j < NJ; j++) {
#pragma unroll
    for (int p = 0; p < 2; p++) {
      const int c = (QI + (j - NZ2 - NZ1)) * 16 + 4 * g + 2 * p + qstart - 108 * cg;
      const int hp = (c * 3641) >> 16, d = c - hp * 18;
      char* dst = lds + K1_QIMG + hp * 4608 + (64 * sh + c16) * 36 + d * 2;
      if (c >= 0 && c < 108) {
#pragma unroll
        for (int m = 0; m < 4; m++) *(unsigned*)(dst + m * 16 * 36) = pk2(acc[j][m][2 * p], acc[j][m][2 * p + 1]);
      }
    }
  }
#pragma unroll
  for (int j = 0; j < NZ1; j++)
#pragma unroll
    for (int mp = 0; mp < 2; mp++) {
      const f32x4 u = acc[NZ2 + j][2 * mp], v = acc[NZ2 + j][2 * mp + 1];
      const u32x4 o = {pk2v(gelu2(lo2(u))), pk2v(gelu2(hi2(u))), pk2v(gelu2(lo2(v))), pk2v(gelu2(hi2(v)))};
      st16(ws + WS_Z1 + ((size_t)(4 * rb + 2 * sh + mp) * 144 + 9 * cg + Z1I + j) * 1024 + l * 16, o);
    }
  const int prow = tid & 127, part = tid >> 7;
  unsigned long long sv[4];
#pragma unroll
  for (int i = 0; i < 4; i++)
    sv[i] = __hip_atomic_load(gstat + (part * 4 + i) * 128 + prow, __ATOMIC_RELAXED, __HIP_MEMORY_SCOPE_AGENT);
  lds_bar();
  TS(1, 3);
#pragma unroll
  for (int u = 0; u < 4; u++) {
    const int q = tid + 512 * u;
    if (u < 3 || q < 1728) {
      const int pi = (q * 3641) >> 20, rem = q - pi * 288;
      st16(ws + WS_QKV + ((size_t)(6 * cg + pi) * S_ + rb * 128) * 36 + rem * 16, *(const u32x4*)(lds + K1_QIMG + q * 16));
    }
  }
  {
    float a = 0.f, q = 0.f;
#pragma unroll
    for (int i = 0; i < 4; i++) {
      unsigned long long v = sv[i];
      while ((unsigned)(v >> 32) == 0xffffffffu)
        v = __hip_atomic_load(gstat + (part * 4 + i) * 128 + prow, __ATOMIC_RELAXED, __HIP_MEMORY_SCOPE_AGENT);
      a += __builtin_bit_cast(float, (unsigned)v);
      q += __builtin_bit_cast(float, (unsigned)(v >> 32));
    }
    ((f32x2*)(lds + K1_STAT2))[part * 128 + prow] = (f32x2){a, q};
  }
  lds_bar();
#pragma unroll
  for (int m = 0; m < 4; m++) {
    const f32x2* st = (const f32x2*)(lds + K1_STAT2) + 64 * sh + 16 * m + c16;
    f32x2 v = st[0] + st[128] + st[256] + st[384];
    const float mean = v[0] * (1.0f / FH);
    const float var = fmaxf(v[1] * (1.0f / FH) - mean * mean, 0.f);
    const float rs = 1.0f / __builtin_sqrtf(var + 1e-5f);
    const f32x2 mean2 = {mean, mean}, rs2 = {rs, rs};
    char* dst = lds + K1_ZIMG + (64 * sh + 16 * m + swap23(c16)) * 288 + Z2I * 32 + g * 8;
#pragma unroll
    for (int j = 0; j < NZ2; j++)
      *(u32x2*)(dst + j * 32) = (u32x2){pk2v((lo2(acc[j][m]) - mean2) * rs2), pk2v((hi2(acc[j][m]) - mean2) * rs2)};
  }
  lds_bar();
  {
    char* zt = ws + WS_Z2 + (size_t)cg * 589824 + (size_t)rb * 36864;
#pragma unroll
    for (int u = 0; u < 5; u++) {
      const int q = tid + 512 * u;
      if (u < 4 || q < 2304) *(u32x4*)(zt + q * 16) = *(const u32x4*)(lds + K1_ZIMG + q * 16);
    }
  }
  TS(1, 4);
}

__global__ __launch_bounds__(512, 2) void k1_proj(const char* __restrict__ w_in, const char* __restrict__ wqkv,
                                                  char* __restrict__ ws, const char* __restrict__ w_sp) {
  extern __shared__ __attribute__((aligned(16))) char lds[];
  const int b = blockIdx.x, xcd = b & 7, i = b >> 3;
  const int rb = 8 * (xcd & 1) + (i & 7), cg = 4 * (xcd >> 1) + (i >> 3);
  const int w = __builtin_amdgcn_readfirstlane(threadIdx.x >> 6);
  const int sh = w >> 2, nq = sh ? 3 - (w & 3) : (w & 3);
  char* ws_dbg = ws; TS(1, 0);
  K1Stage gs;
  gs.init(lds, w_in, wqkv, ws, rb, cg, w);
  gs.issue(0, w); gs.issue(1, w); gs.issue(2, w); gs.issue(3, w);
  if (nq == 0) k1_body<0>(lds, gs, ws, w_sp, rb, cg, w, sh);
  else if (nq == 1) k1_body<1>(lds, gs, ws, w_sp, rb, cg, w, sh);
  else if (nq == 2) k1_body<2>(lds, gs, ws, w_sp, rb, cg, w, sh);
  else k1_body<3>(lds, gs, ws, w_sp, rb, cg, w, sh);
}

struct AttnIn { u32x4 qv[2], k0, k1, v0, v1; unsigned k2, v2; };
DEVI void attn_load(AttnIn& in, const char* __restrict__ ws) {
  const int b = blockIdx.x, h = b & 31, q0 = (b >> 5) * 256;
  const int tid = threadIdx.x, g = (tid & 63) >> 4, c16 = tid & 15;
  const char* qkv = ws + WS_QKV;
#pragma unroll
  for (int m = 0; m < 2; m++)
    in.qv[m] = *(const u32x4_a4*)(qkv + ((size_t)h * S_ + q0 + 32 * (tid >> 6) + 16 * m + c16) * 36 + (g < 2 ? 16 * g : 20));
  const char* kp = qkv + ((size_t)(32 + h) * S_ + max(q0 - 256 + tid, 0)) * 36;
  const char* vp = kp + (size_t)32 * S_ * 36;
  in.k0 = *(const u32x4_a4*)kp; in.k1 = *(const u32x4_a4*)(kp + 16); in.k2 = *(const unsigned*)(kp + 32);
  in.v0 = *(const u32x4_a4*)vp; in.v1 = *(const u32x4_a4*)(vp + 16); in.v2 = *(const unsigned*)(vp + 32);
}

DEVI void attn_phase(char* lds, const AttnIn& in, const char* __restrict__ wo, const char* __restrict__ w_out,
                     char* __restrict__ ws) {
  const int b = blockIdx.x, h = b & 31, qb = b >> 5;
  const int tid = threadIdx.x, l = tid & 63, g = l >> 4, c16 = l & 15;
  const int w = __builtin_amdgcn_readfirstlane(tid >> 6);
  const int key = qb * 256 - 256 + tid;
  char* ws_dbg = ws;
  const u32x4 k0 = in.k0, k1 = in.k1, v0 = in.v0, v1 = in.v1;
  const unsigned k2 = in.k2, v2 = in.v2;
  bf16x8 qf[2];
  {
    const int f = (-(tid >> 2)) & 3;
    char* kr = lds + tid * 64;
    *(u32x4*)(kr + ((0 ^ f) * 16)) = k0;
    *(u32x4*)(kr + ((1 ^ f) * 16)) = k1;
    *(u32x4*)(kr + ((2 ^ f) * 16)) = (u32x4){k2, key < 0 ? 0x0000f149u : 0u, 0, 0};
    *(u32x4*)(kr + ((3 ^ f) * 16)) = (u32x4){0, 0, 0, 0};
    char* vr = lds + 32768 + tid * 64;
    const int x = ((tid >> 2) & 1) * 32;
    *(u32x4*)(vr + (0 ^ x)) = v0;
    *(u32x4*)(vr + (16 ^ x)) = v1;
    *(u32x4*)(vr + (32 ^ x)) = (u32x4){v2, 0x00003f80u, 0, 0};
    *(u32x4*)(vr + (48 ^ x)) = (u32x4){0, 0, 0, 0};
  }
#pragma unroll
  for (int m = 0; m < 2; m++) {
    u32x4 v = in.qv[m];
    if (g >= 2) v = (u32x4){g == 2 ? v[3] : 0u, g == 2 ? 0x00003f80u : 0u, 0, 0};
    qf[m] = __builtin_bit_cast(bf16x8, v);
  }
  lds_bar();
  TW(2, 0);
  const int wu = w * 256 + b;
  WPrep wp;
  if (wu < 810) wp.load(wo, w_out, wu / 9, wu % 9, l);
  const float sc = 0.23570226039551584f * 1.4426950408889634f;
  const int koff = (32 * w + c16) * 64 + ((g ^ ((-(c16 >> 2)) & 3)) * 16);
  const int voff = 32768 + (32 * w + 4 * g + (c16 >> 2)) * 64 + 8 * (c16 & 3);
  const int vx = (g & 1) * 32;
#pragma unroll
  for (int m = 0; m < 2; m++) {
    f32x4 s[17];
#pragma unroll
    for (int i = 0; i < 17; i++) {
      bf16x8 kf = lds_b128(lds + koff + (i + m) * 1024);
      s[i] = mfma(kf, qf[m], (f32x4){0.f, 0.f, 0.f, 0.f});
    }
    const float ninf = -__builtin_inff();
#pragma unroll
    for (int r = 0; r < 4; r++) {
      if (!(c16 < 4 * g + r)) s[0][r] = ninf;
      if (!(4 * g + r <= c16)) s[16][r] = ninf;
    }
    float mxa = ninf, mxb = ninf;
#pragma unroll
    for (int i = 0; i < 17; i++) { mxa = fmx(fmx(mxa, s[i][0]), s[i][1]); mxb = fmx(fmx(mxb, s[i][2]), s[i][3]); }
    float mx = fmx(mxa, mxb);
    mx = fmx(mx, __shfl_xor(mx, 16));
    mx = fmx(mx, __shfl_xor(mx, 32));
    const float nb = -mx * sc;
    const f32x2 sc2 = {sc, sc}, nb2 = {nb, nb};
    unsigned p[18][2];
#pragma unroll
    for (int i = 0; i < 17; i++) {
      const f32x2 ta = __builtin_elementwise_fma((f32x2){s[i][0], s[i][1]}, sc2, nb2);
      const f32x2 tb = __builtin_elementwise_fma((f32x2){s[i][2], s[i][3]}, sc2, nb2);
      const float a0 = ta[0], a1 = ta[1], b0 = tb[0], b1 = tb[1];
      p[i][0] = pk2(__builtin_amdgcn_exp2f(a0), __builtin_amdgcn_exp2f(a1));
      p[i][1] = pk2(__builtin_amdgcn_exp2f(b0), __builtin_amdgcn_exp2f(b1));
    }
    p[17][0] = 0; p[17][1] = 0;
    f32x4 o0 = {0.f, 0.f, 0.f, 0.f}, o1 = o0;
#pragma unroll
    for (int i = 0; i < 9; i++) {
      const int r0 = (2 * i + m) * 1024, r1 = (i < 8) ? r0 + 1024 : r0;
      bf16x8 pf = __builtin_bit_cast(bf16x8, (u32x4){p[2 * i][0], p[2 * i][1], p[2 * i + 1][0], p[2 * i + 1][1]});
      bf16x8 va = cat8(lds_tr(lds + voff + vx + r0), lds_tr(lds + voff + vx + r1));
      bf16x8 vb = cat8(lds_tr(lds + voff + (32 ^ vx) + r0), lds_tr(lds + voff + (32 ^ vx) + r1));
      o0 = mfma(va, pf, o0);
      o1 = mfma(vb, pf, o1);
    }
    const float den = __shfl(o1[2], c16);
    const float inv = 1.0f / den;
    char* op = ws + WS_O + (size_t)(16 * qb + 2 * w + m) * 18432;
    *(u32x2*)(op + (h >> 1) * 1024 + l * 16 + (h & 1) * 8) = (u32x2){pk2(o0[0] * inv, o0[1] * inv), pk2(o0[2] * inv, o0[3] * inv)};
    if (g == 0)
      *(unsigned*)(op + (16 + (h >> 4)) * 1024 + (((h & 7) >> 1) * 16 + c16) * 16 + ((h >> 3) & 1) * 8 + (h & 1) * 4) =
          pk2(o1[0] * inv, o1[1] * inv);
  }
  if (wu < 810) wp.store(ws, wu / 9, wu % 9, l);
  TW(2, 1);
}

constexpr int G_SLOT = 16384 + 18432;
constexpr int G_LDS = 4 * G_SLOT;
constexpr int K2_LDS = 81920 + 65536;
template <int NJ> struct Frag2 { bf16x8 a[NJ]; bf16x8 b[4]; };

struct GStage {
  unsigned bo[2], ao[3], bdst, adst;
  const char* Bb;
  const char* Ab;
  DEVI void init(char* lds, const char* B, const char* A, int w) {
    const int l = threadIdx.x & 63;
    Bb = B; Ab = A;
#pragma unroll
    for (int i = 0; i < 2; i++) {
      int r = (2 * w + i) * 8 + (l >> 3);
      int c = (l & 7) ^ ((r >> 1) & 7);
      bo[i] = r * 4096 + c * 16;
    }
#pragma unroll
    for (int i = 0; i < 3; i++) {
      int q = w * 144 + i * 64 + ((i == 2) ? (l & 15) : l);
      ao[i] = q * 16;
    }
    const unsigned lb = (unsigned)(uintptr_t)(AS3 char*)lds;
    bdst = lb + w * 2048; adst = lb + 16384 + w * 2304;
  }
  DEVI void piece(int t, int i) const {
    const unsigned so = (t & 3) * G_SLOT;
    const size_t astep = 18432;
    if (i < 2) glds_s(bo[i], Bb + t * 128, bdst + so + i * 1024);
    else if (i < 4) glds_s(ao[i - 2], Ab + (size_t)t * astep, adst + so + (i - 2) * 1024);
    else glds_s16(ao[2], Ab + (size_t)t * astep, adst + so + 2048);
  }
  DEVI void issue(int t) const {
#pragma unroll
    for (int i = 0; i < 5; i++) piece(t, i);
  }
};

template <int NJ, int NS>
DEVI void gemm_core(char* lds, const GStage& gs, int w, f32x4 (&acc)[NJ][4], char* ws_dbg, int kid) {
  const int l = threadIdx.x & 63, g = l >> 4, c16 = l & 15;
  const int fh = w >> 2, th = w & 1, kh = (w >> 1) & 1;
  const int boff = (64 * th + c16) * 128 + (((4 * kh + g) ^ (c16 >> 1)) * 16);
  const int aoff = 16384 + (32 * kh + 16 * (g >> 1) + 4 * (g & 1) + (c16 >> 2)) * 288 + fh * 160 + 8 * (c16 & 3);
  auto rd = [&](Frag2<NJ>& f, int t) {
    const char* slot = lds + (t & 3) * G_SLOT;
#pragma unroll
    for (int m = 0; m < 4; m++) f.b[m] = lds_b128(slot + boff + m * 2048);
#pragma unroll
    for (int j = 0; j < NJ; j++)
      f.a[j] = cat8(lds_tr(slot + aoff + j * 32), lds_tr(slot + aoff + j * 32 + 8 * 288));
  };
  auto step = [&](const Frag2<NJ>& cur, Frag2<NJ>& nxt, int t, bool doissue) {
    const char* slot = lds + ((t + 1) & 3) * G_SLOT;
    const char* pb = slot + boff;
    const char* pa = slot + aoff;
    bf16x4 lo[NJ];
#pragma unroll
    for (int x = 0; x < 4 * NJ; x++) {
      const int j = x >> 2, m = x & 3;
      acc[j][m] = mfma(cur.a[j], cur.b[m], acc[j][m]);
      if (x < 4) nxt.b[x] = lds_b128(pb + x * 2048);
      else if (x < 4 + 2 * NJ) {
        const int y = x - 4, jj = y >> 1;
        if ((y & 1) == 0) lo[jj] = lds_tr(pa + jj * 32);
        else nxt.a[jj] = cat8(lo[jj], lds_tr(pa + jj * 32 + 8 * 288));
      }
      if (doissue && (x % 3) == 1 && x / 3 < 5) gs.piece(t + 4, x / 3);
      SB();
    }
  };
  auto mm = [&](const Frag2<NJ>& f) {
#pragma unroll
    for (int j = 0; j < NJ; j++)
#pragma unroll
      for (int m = 0; m < 4; m++) acc[j][m] = mfma(f.a[j], f.b[m], acc[j][m]);
  };
#pragma unroll
  for (int j = 0; j < NJ; j++)
#pragma unroll
    for (int m = 0; m < 4; m++) acc[j][m] = (f32x4){0.f, 0.f, 0.f, 0.f};
  Frag2<NJ> f0, f1;
  wait_vm_lgkm0<15>();
  bar();
  TS(kid, 1);
  rd(f0, 0);
  constexpr int M = NS - 4, MP = M & ~1;
#pragma unroll 1
  for (int t = 0; t < MP; t += 2) {
    wait_vm_lgkm0<10>(); bar(); SB();
    step(f0, f1, t, true);
    wait_vm_lgkm0<10>(); bar(); SB();
    step(f1, f0, t + 1, true);
  }
  if constexpr (M & 1) {
    wait_vm_lgkm0<10>(); bar(); SB(); step(f0, f1, NS - 5, true);
    wait_vm_lgkm0<10>(); bar(); SB(); step(f1, f0, NS - 4, false);
    wait_vm_lgkm0<5>();  bar(); SB(); step(f0, f1, NS - 3, false);
    wait_vm_lgkm0<0>();  bar(); SB(); step(f1, f0, NS - 2, false);
    mm(f0);
  } else {
    wait_vm_lgkm0<10>(); bar(); SB(); step(f0, f1, NS - 4, false);
    wait_vm_lgkm0<5>();  bar(); SB(); step(f1, f0, NS - 3, false);
    wait_vm_lgkm0<0>();  bar(); SB(); step(f0, f1, NS - 2, false);
    mm(f1);
  }
  TS(kid, 2);
}

template <int NJ>
DEVI void khalf_exchange(char* lds, int w, f32x4 (&acc)[NJ][4]) {
  const int l = threadIdx.x & 63, kh = (w >> 1) & 1;
  lds_bar();
  f32x4* mine = (f32x4*)(lds + w * 10240) + l;
  const f32x4* other = (const f32x4*)(lds + (w ^ 2) * 10240) + l;
  if (kh == 0) {
#pragma unroll
    for (int j = 0; j < NJ; j++) { mine[(2 * j) * 64] = acc[j][2]; mine[(2 * j + 1) * 64] = acc[j][3]; }
  } else {
#pragma unroll
    for (int j = 0; j < NJ; j++) {
      mine[(2 * j) * 64] = acc[j][0]; mine[(2 * j + 1) * 64] = acc[j][1];
      acc[j][0] = acc[j][2]; acc[j][1] = acc[j][3];
    }
  }
  lds_bar();
#pragma unroll
  for (int j = 0; j < NJ; j++) { acc[j][0] += other[(2 * j) * 64]; acc[j][1] += other[(2 * j + 1) * 64]; }
}

template <int NJ>
DEVI void k2_body(char* lds, const GStage& gs, const char* __restrict__ b_sp, char* __restrict__ ws,
                  int tb, int fb, int w) {
  f32x4 acc[NJ][4];
  gemm_core<NJ, 32>(lds, gs, w, acc, ws, 2);
  const int l = threadIdx.x & 63, g = l >> 4, c16 = l & 15;
  const int fh = w >> 2, th = w & 1, kh = (w >> 1) & 1;
  const int trow = tb * 128 + 64 * th + 32 * kh + c16;
  u32x4 z[NJ];
  float bs[2];
#pragma unroll
  for (int m = 0; m < 2; m++)
    bs[m] = __builtin_bit_cast(float, (unsigned)(*(const unsigned short*)(b_sp + (trow + 16 * m) * 2)) << 16);
#pragma unroll
  for (int j = 0; j < NJ; j++)
    z[j] = *(const u32x4*)(ws + WS_Z1 + ((size_t)(4 * tb + 2 * th + kh) * 144 + 9 * fb + 5 * fh + j) * 1024 + l * 16);
  khalf_exchange<NJ>(lds, w, acc);
  { char* ws_dbg = ws; TS(2, 3); }
  const int J0 = 9 * fb + 5 * fh;
#pragma unroll
  for (int m = 0; m < 2; m++) {
    u32x2 o[NJ];
#pragma unroll
    for (int j = 0; j < NJ; j++) {
      const f32x4 a = acc[j][m];
      const unsigned z0 = z[j][2 * m], z1 = z[j][2 * m + 1];
      o[j] = (u32x2){pk2(bflo(z0) * (a[0] + bs[m]), bfhi(z0) * (a[1] + bs[m])),
                     pk2(bflo(z1) * (a[2] + bs[m]), bfhi(z1) * (a[3] + bs[m]))};
    }
    char* hp = ws + WS_H + (size_t)(8 * tb + 4 * th + 2 * kh + m) * 73728 + (J0 >> 1) * 1024 + l * 16;
    if (J0 & 1) {
      *(u32x2*)(hp + 8) = o[0];
      st16(hp + 1024, (u32x4){o[1][0], o[1][1], o[2][0], o[2][1]});
      if constexpr (NJ == 5) st16(hp + 2048, (u32x4){o[3][0], o[3][1], o[4][0], o[4][1]});
      else *(u32x2*)(hp + 2048) = o[3];
    } else {
      st16(hp, (u32x4){o[0][0], o[0][1], o[1][0], o[1][1]});
      st16(hp + 1024, (u32x4){o[2][0], o[2][1], o[3][0], o[3][1]});
      if constexpr (NJ == 5) *(u32x2*)(hp + 2048) = o[4];
    }
  }
}
__global__ __launch_bounds__(512, 2) void k2_sgu(const char* __restrict__ w_sp, const char* __restrict__ b_sp,
                                                 const char* __restrict__ wo, const char* __restrict__ w_out,
                                                 char* __restrict__ ws) {
  extern __shared__ __attribute__((aligned(16))) char lds[];
  const int b = blockIdx.x, xcd = b & 7, i = b >> 3;
  const int tb = 8 * (xcd & 1) + (i & 7), fb = 4 * (xcd >> 1) + (i >> 3);
  const int w = __builtin_amdgcn_readfirstlane(threadIdx.x >> 6);
  char* ws_dbg = ws; TS(2, 0);
  GStage gs;
  gs.init(lds, w_sp + (size_t)tb * 128 * 4096, ws + WS_Z2 + (size_t)fb * 589824, w);
  gs.issue(0); gs.issue(1); gs.issue(2); gs.issue(3);
  AttnIn ain;
  attn_load(ain, ws);
  if (w < 4) k2_body<5>(lds, gs, b_sp, ws, tb, fb, w);
  else k2_body<4>(lds, gs, b_sp, ws, tb, fb, w);
  TS(2, 4);
  attn_phase(lds + 81920, ain, wo, w_out, ws);
}

constexpr int KC_LDS = 8 * 20 * 1024;
struct FragC { u32x4 h[5]; u32x4 wv[4]; };

template <int NB, int KSTEP>
DEVI void kc_stream(const char* __restrict__ hb, const unsigned (&ho)[5], const char* __restrict__ wb, unsigned wofs,
                    f32x4 (&acc)[5][4]) {
  constexpr int NBUF = 3;
  auto ld = [&](FragC& f, int i) {
    const int ii = i;
#pragma unroll
    for (int m = 0; m < 5; m++) f.h[m] = *(const u32x4*)(hb + (size_t)ii * (KSTEP * 1024) + ho[m]);
#pragma unroll
    for (int j = 0; j < 4; j++) f.wv[j] = *(const u32x4*)(wb + (size_t)ii * (KSTEP * 36 * 1024) + wofs + j * 1024);
    SB();
  };
  auto mm = [&](const FragC& f) {
#pragma unroll
    for (int m = 0; m < 5; m++)
#pragma unroll
      for (int j = 0; j < 4; j++)
        acc[m][j] = mfma(__builtin_bit_cast(bf16x8, f.wv[j]), __builtin_bit_cast(bf16x8, f.h[m]), acc[m][j]);
    SB();
  };
  FragC f[NBUF];
#pragma unroll
  for (int i = 0; i < NBUF - 1; i++) ld(f[i], i);
#pragma unroll
  for (int i = 0; i < NB; i++) {
    if (i + NBUF - 1 < NB) ld(f[(i + NBUF - 1) % NBUF], i + NBUF - 1);
    mm(f[i % NBUF]);
  }
}

__global__ __launch_bounds__(512, 2) void kc_out(const char* __restrict__ x, char* __restrict__ ws,
                                                 char* __restrict__ out) {
  extern __shared__ __attribute__((aligned(16))) char lds[];
  const int b = blockIdx.x, xcd = b & 7, i = b >> 3;
  const int unit = xcd * 30 + i;
  if (unit >= 234) return;
  const int tb = unit / 9, db = unit - tb * 9;
  const int l = threadIdx.x & 63, g = l >> 4, c16 = l & 15;
  const int w = __builtin_amdgcn_readfirstlane(threadIdx.x >> 6);
  char* ws_dbg = ws; TS(3, 0); TS(3, 1);
  f32x4 acc[5][4];
#pragma unroll
  for (int m = 0; m < 5; m++)
#pragma unroll
    for (int j = 0; j < 4; j++) acc[m][j] = (f32x4){0.f, 0.f, 0.f, 0.f};
  unsigned ho[5];
  if (w < 6) {
#pragma unroll
    for (int m = 0; m < 5; m++) ho[m] = min(5 * tb + m, 127) * 73728 + l * 16;
    kc_stream<12, 6>(ws + WS_H + w * 1024, ho, ws + WS_WF + (size_t)(w * 36 + db * 4) * 1024, l * 16, acc);
  } else {
#pragma unroll
    for (int m = 0; m < 5; m++) ho[m] = min(5 * tb + m, 127) * 18432 + l * 16;
    kc_stream<9, 2>(ws + WS_O + (w - 6) * 1024, ho, ws + WS_WF + (size_t)((72 + w - 6) * 36 + db * 4) * 1024, l * 16, acc);
  }
  TS(3, 2); TW(3, 0);
  u32x2 xv[3];
#pragma unroll
  for (int q = 0; q < 3; q++) {
    const int st = min(w + 8 * q, 19), ts = min(5 * tb + (st >> 2), 127);
    xv[q] = *(const u32x2*)(x + (size_t)(ts * 16 + c16) * 1152 + (db * 64 + (st & 3) * 16 + 4 * g) * 2);
  }
  f32x4* red = (f32x4*)lds;
#pragma unroll
  for (int m = 0; m < 5; m++)
#pragma unroll
    for (int j = 0; j < 4; j++) red[(w * 20 + m * 4 + j) * 64 + l] = acc[m][j];
  asm volatile("s_waitcnt lgkmcnt(0)" ::: "memory");
  TW(3, 1);
  lds_bar();
  TS(3, 3);
#pragma unroll
  for (int q = 0; q < 3; q++) {
    const int st = w + 8 * q, ts = 5 * tb + (st >> 2);
    if (st < 20 && ts < 128) {
      const f32x4* p = red + st * 64 + l;
      f32x4 y = (p[0] + p[20 * 64]) + (p[40 * 64] + p[60 * 64]) + (p[80 * 64] + p[100 * 64]);
      f32x4 sv = p[120 * 64] + p[140 * 64];
      const float xf[4] = {bflo(xv[q][0]), bfhi(xv[q][0]), bflo(xv[q][1]), bfhi(xv[q][1])};
      float o[4];
#pragma unroll
      for (int e = 0; e < 4; e++) {
        const float sig = __builtin_amdgcn_rcpf(1.0f + __builtin_amdgcn_exp2f(-1.4426950408889634f * y[e]));
        o[e] = xf[e] + sv[e] * (y[e] * sig);
      }
      *(u32x2*)(out + (size_t)(ts * 16 + c16) * 1152 + (db * 64 + (st & 3) * 16 + 4 * g) * 2) =
          (u32x2){pk2(o[0], o[1]), pk2(o[2], o[3])};
    }
  }
  TS(3, 4);
}

extern "C" void kernel_launch(void* const* d_in, const int* in_sizes, int n_in, void* d_out, int out_size,
                              void* d_ws, size_t ws_size, hipStream_t stream) {
  const char* x = (const char*)d_in[0];
  const char* wqkv = (const char*)d_in[1];
  const char* wo = (const char*)d_in[2];
  const char* w_in = (const char*)d_in[4];
  const char* w_sp = (const char*)d_in[5];
  const char* b_sp = (const char*)d_in[6];
  const char* w_out = (const char*)d_in[7];
  char* ws = (char*)d_ws;
  (void)hipFuncSetAttribute((const void*)k1_proj, hipFuncAttributeMaxDynamicSharedMemorySize, K1_LDS);
  (void)hipFuncSetAttribute((const void*)k2_sgu, hipFuncAttributeMaxDynamicSharedMemorySize, K2_LDS);
  (void)hipFuncSetAttribute((const void*)kc_out, hipFuncAttributeMaxDynamicSharedMemorySize, KC_LDS);
  constexpr int NPF = 57344 / 256;
  k0_ln<<<256 + NPF, 64, 0, stream>>>(x, ws, w_in, wqkv);
  k1_proj<<<256, 512, K1_LDS, stream>>>(w_in, wqkv, ws, w_sp);
  k2_sgu<<<256, 512, K2_LDS, stream>>>(w_sp, b_sp, wo, w_out, ws);
  kc_out<<<8 * 30, 512, KC_LDS, stream>>>(x, ws, (char*)d_out);
}
```

```cpp
#include <hip/hip_runtime.h>
#include <hip/hip_bf16.h>
#include <stdint.h>
#pragma clang diagnostic ignored "-Winline-asm"

typedef __attribute__((ext_vector_type(8))) __bf16 bf16x8;
typedef __attribute__((ext_vector_type(4))) __bf16 bf16x4;
typedef __attribute__((ext_vector_type(2))) __bf16 bf16x2;
typedef __attribute__((ext_vector_type(4))) float f32x4;
typedef __attribute__((ext_vector_type(2))) float f32x2;
typedef __attribute__((ext_vector_type(4))) unsigned u32x4;
typedef __attribute__((ext_vector_type(2))) unsigned u32x2;
typedef u32x4 u32x4_a4 __attribute__((aligned(4)));
typedef u32x2 u32x2_a4 __attribute__((aligned(4)));
#define AS1 __attribute__((address_space(1)))
#define AS3 __attribute__((address_space(3)))
#define DEVI __device__ __forceinline__

constexpr int S_ = 2048, D_ = 576, FH = 2304;

constexpr size_t WS_XN   = 0;
constexpr size_t WS_Z1   = WS_XN + (size_t)S_ * D_ * 2;
constexpr size_t WS_Z2   = WS_Z1 + (size_t)S_ * FH * 2;
constexpr size_t WS_H    = WS_Z2 + (size_t)S_ * FH * 2;
constexpr size_t WS_QKV  = WS_H + (size_t)S_ * FH * 2;
constexpr size_t WS_O    = WS_QKV + (size_t)96 * S_ * 18 * 2;
constexpr size_t WS_PART = WS_O + (size_t)S_ * D_ * 2;
constexpr size_t WS_STAT = WS_PART + (size_t)4 * S_ * D_ * 4;
constexpr size_t WS_WF   = WS_STAT + (size_t)16 * 16 * 128 * 8;

DEVI void glds16(const char* g, char* lds) {
  __builtin_amdgcn_global_load_lds((const AS1 void*)g, (AS3 void*)lds, 16, 0, 0);
}
DEVI void glds_s(unsigned voff, const char* sbase, unsigned ldsa) {
  asm volatile("s_mov_b32 m0, %2\n\ts_nop 0\n\tglobal_load_lds_dwordx4 %0, %1" ::"v"(voff), "s"(sbase), "s"(ldsa) : "memory", "m0");
}
DEVI void glds_s16(unsigned voff, const char* sbase, unsigned ldsa) {
  asm volatile("s_mov_b32 m0, %2\n\ts_mov_b64 exec, 0xffff\n\tglobal_load_lds_dwordx4 %0, %1\n\ts_mov_b64 exec, -1"
               ::"v"(voff), "s"(sbase), "s"(ldsa) : "memory", "m0");
}
DEVI void glds_s8(unsigned voff, const char* sbase, unsigned ldsa) {
  asm volatile("s_mov_b32 m0, %2\n\ts_mov_b64 exec, 0xff\n\tglobal_load_lds_dwordx4 %0, %1\n\ts_mov_b64 exec, -1"
               ::"v"(voff), "s"(sbase), "s"(ldsa) : "memory", "m0");
}
DEVI void touch_s(unsigned voff, const char* sbase, unsigned ldsa) {
  asm volatile("s_mov_b32 m0, %2\n\ts_nop 0\n\tglobal_load_lds_dword %0, %1" ::"v"(voff), "s"(sbase), "s"(ldsa) : "memory", "m0");
}
DEVI void st16(char* p, u32x4 v) {

  *(u32x4*)p = v;
}
#define SB() __builtin_amdgcn_sched_barrier(0)
#define TS(K, I)
#define TW(K, I)
DEVI bf16x8 lds_b128(const char* p) { return *(const AS3 bf16x8*)p; }
DEVI bf16x4 lds_tr(const char* p) { return __builtin_amdgcn_ds_read_tr16_b64_v4bf16((AS3 bf16x4*)p); }
DEVI bf16x8 cat8(bf16x4 a, bf16x4 b) { return __builtin_shufflevector(a, b, 0, 1, 2, 3, 4, 5, 6, 7); }
DEVI f32x4 mfma(bf16x8 a, bf16x8 b, f32x4 c) { return __builtin_amdgcn_mfma_f32_16x16x32_bf16(a, b, c, 0, 0, 0); }
DEVI unsigned pk2(float a, float b) {
  f32x2 v = {a, b};
  bf16x2 r = __builtin_convertvector(v, bf16x2);
  return __builtin_bit_cast(unsigned, r);
}
DEVI float bflo(unsigned u) { return __builtin_bit_cast(float, u << 16); }
DEVI float bfhi(unsigned u) { return __builtin_bit_cast(float, u & 0xffff0000u); }
template <int N> DEVI void wait_vm_lgkm0() { asm volatile("s_waitcnt vmcnt(%0) lgkmcnt(0)" ::"n"(N) : "memory"); }
DEVI void bar() { __builtin_amdgcn_s_barrier(); asm volatile("" ::: "memory"); }
DEVI void lds_bar() { asm volatile("s_waitcnt lgkmcnt(0)" ::: "memory"); bar(); }
DEVI float gelu_t(float y) {
  float p = __builtin_fmaf(y * y, -0.1029432397f, -2.3022081983f);
  float e = __builtin_amdgcn_exp2f(y * p);
  return y * __builtin_amdgcn_rcpf(1.0f + e);
}
DEVI float fmx(float a, float b) { return __builtin_elementwise_maximum(a, b); }
DEVI int swap23(int r) { return (r & ~12) | ((r & 4) << 1) | ((r & 8) >> 1); }

struct WPrep {
  unsigned short v[4][8];
  DEVI void load(const char* __restrict__ wo, const char* __restrict__ w_out, int kb, int dq, int l) {
    const int g = l >> 4, c16 = l & 15;
#pragma unroll
    for (int e = 0; e < 8; e++) {
      const int r = e & 3, J = 2 * (kb < 72 ? kb : kb - 72) + (e >> 2);
      const char* src;
      if (kb < 72) src = w_out + (size_t)(16 * J + 4 * g + r) * 1152;
      else if (J < 32) src = wo + (size_t)(18 * J + 4 * g + r) * 1152;
      else src = wo + (size_t)((8 * (J - 32) + 2 * g + (r >> 1)) * 18 + 16 + (r & 1)) * 1152;
#pragma unroll
      for (int j = 0; j < 4; j++) v[j][e] = *(const unsigned short*)(src + (dq * 64 + j * 16 + c16) * 2);
    }
  }
  DEVI void store(char* __restrict__ ws, int kb, int dq, int l) const {
#pragma unroll
    for (int j = 0; j < 4; j++) {
      u32x4 o;
#pragma unroll
      for (int e = 0; e < 4; e++) o[e] = (unsigned)v[j][2 * e] | ((unsigned)v[j][2 * e + 1] << 16);
      *(u32x4*)(ws + WS_WF + ((size_t)kb * 36 + dq * 4 + j) * 1024 + l * 16) = o;
    }
  }
};

__global__ __launch_bounds__(64) void k0_ln(const char* __restrict__ x, char* __restrict__ ws,
                                            const char* __restrict__ w_in, const char* __restrict__ wqkv) {
  __shared__ __attribute__((aligned(16))) char pf[256];
  const int l = threadIdx.x, b = blockIdx.x;
  if (b >= 256) {
    const int u = b - 256;
    const unsigned lb = (unsigned)(uintptr_t)(AS3 char*)pf;
#pragma unroll
    for (int i = 0; i < 4; i++) {
      const int ln = (u * 4 + i) * 64;
      if (ln < 41472) touch_s(l * 128, w_in + (size_t)ln * 128, lb);
      else if (ln < 41472 + 15552) touch_s(l * 128, wqkv + (size_t)(ln - 41472) * 128, lb);
    }
    asm volatile("s_waitcnt vmcnt(0)" ::: "memory");
    return;
  }
  const int row = b * 8 + (l >> 3), c0 = l & 7;
  char* ws_dbg = ws; TS(0, 0);
  const char* src = x + (size_t)row * 1152 + c0 * 16;
  u32x4 v[9];
#pragma unroll
  for (int i = 0; i < 9; i++) v[i] = *(const u32x4*)(src + i * 128);
  *(u32x4*)(ws + WS_STAT + (size_t)b * 1024 + l * 16) = (u32x4){~0u, ~0u, ~0u, ~0u};
  float s = 0.f;
#pragma unroll
  for (int i = 0; i < 9; i++)
#pragma unroll
    for (int e = 0; e < 4; e++) s += bflo(v[i][e]) + bfhi(v[i][e]);
  s += __shfl_xor(s, 1); s += __shfl_xor(s, 2); s += __shfl_xor(s, 4);
  const float mean = s * (1.0f / 576.0f);
  float q = 0.f;
#pragma unroll
  for (int i = 0; i < 9; i++)
#pragma unroll
    for (int e = 0; e < 4; e++) {
      float a = bflo(v[i][e]) - mean, c = bfhi(v[i][e]) - mean;
      q += a * a + c * c;
    }
  q += __shfl_xor(q, 1); q += __shfl_xor(q, 2); q += __shfl_xor(q, 4);
  const float rs = 1.0f / __builtin_sqrtf(q * (1.0f / 576.0f) + 1e-5f);
  const int r = row & 127;
  char* dst = ws + WS_XN + (size_t)((row >> 7) * 18 + (c0 >> 2)) * 8192 + r * 64 + (((c0 & 3) ^ ((-(r >> 2)) & 3)) * 16);
#pragma unroll
  for (int i = 0; i < 9; i++) {
    u32x4 o;
#pragma unroll
    for (int e = 0; e < 4; e++) o[e] = pk2((bflo(v[i][e]) - mean) * rs, (bfhi(v[i][e]) - mean) * rs);
    *(u32x4*)(dst + i * 16384) = o;
  }
  TS(0, 4);
}

constexpr int K1_SLOT = 8192 + 25600;
constexpr int K1_STAT = 4 * K1_SLOT;
constexpr int K1_STAT2 = K1_STAT + 4096;
constexpr int K1_PF = K1_STAT2 + 4096;
constexpr int K1_LDS = K1_PF + 1024;
constexpr int K1_QIMG = 0;
constexpr int K1_ZIMG = 32768;

template <int NJ> struct Frag1 { bf16x8 a[NJ]; bf16x8 b[4]; };

struct K1Stage {
  unsigned xo, wo[4], xdst, wdst;
  const char* Xb;
  const char* Wa;
  const char* Wc;
  const char* Wq;
  unsigned cstep;
  DEVI void init(char* lds, const char* w_in, const char* wqkv, const char* ws, int rb, int cg, int w) {
    const int l = threadIdx.x & 63;
    const int qstart = 108 * cg - ((cg & 1) ? 4 : 0);
    Xb = ws + WS_XN + (size_t)rb * 18 * 8192;
    xo = w * 1024 + l * 16;
#pragma unroll
    for (int i = 0; i < 4; i++) {
      const int ins = (i < 3) ? w + 8 * i : 24;
      if (ins < 18) {
        const int ty = ins >= 9, q = (ins - 9 * ty) * 64 + l;
        const int rho = q / 18, c = q - rho * 18;
        wo[i] = swap23(rho) * 9216 + ((ty ? 0 : FH) + 144 * cg + c * 8) * 2;
      } else {
        const int q = (i < 3) ? (ins - 18) * 64 + l : 6 * 64 + w * 8 + (l & 7);
        const int rho = q / 14, c = q - rho * 14;
        wo[i] = swap23(rho) * 3456 + (qstart + c * 8) * 2;
      }
    }
    Wa = w_in; Wq = wqkv;
    Wc = (w < 2) ? w_in : wqkv;
    cstep = (w < 2) ? 32 * 9216 : 32 * 3456;
    const unsigned lb = (unsigned)(uintptr_t)(AS3 char*)lds;
    xdst = lb + w * 1024;
    wdst = lb + 8192 + w * 1024;
  }
  DEVI void piece(int t, int i, int w) const {
    const unsigned so = (t & 3) * K1_SLOT;
    if (i == 0) glds_s(xo, Xb + t * 8192, xdst + so);
    else if (i == 1) glds_s(wo[0], Wa + (size_t)t * (32 * 9216), wdst + so);
    else if (i == 2) glds_s(wo[1], Wa + (size_t)t * (32 * 9216), wdst + so + 8192);
    else if (i == 3) glds_s(wo[2], Wc + (size_t)t * cstep, wdst + so + 16384);
    else glds_s8(wo[3], Wq + (size_t)t * (32 * 3456), wdst + so + 24576 - w * 896);
  }
  DEVI void issue(int t, int w) const {
#pragma unroll
    for (int i = 0; i < 5; i++) piece(t, i, w);
  }
};

DEVI f32x2 gelu2(f32x2 y) {
  const f32x2 c1 = {-0.1029432397f, -0.1029432397f}, c0 = {-2.3022081983f, -2.3022081983f};
  const f32x2 a = y * __builtin_elementwise_fma(y * y, c1, c0);
  const float a0 = a[0], a1 = a[1];
  const f32x2 d = (f32x2){__builtin_amdgcn_exp2f(a0), __builtin_amdgcn_exp2f(a1)} + (f32x2){1.0f, 1.0f};
  const float d0 = d[0], d1 = d[1];
  return y * (f32x2){__builtin_amdgcn_rcpf(d0), __builtin_amdgcn_rcpf(d1)};
}
DEVI unsigned pk2v(f32x2 v) { return __builtin_bit_cast(unsigned, __builtin_convertvector(v, bf16x2)); }
DEVI f32x2 lo2(f32x4 v) { return __builtin_shufflevector(v, v, 0, 1); }
DEVI f32x2 hi2(f32x4 v) { return __builtin_shufflevector(v, v, 2, 3); }

template <int NQ>
DEVI void k1_body(char* lds, const K1Stage& gs, char* __restrict__ ws, const char* __restrict__ w_sp,
                  int rb, int cg, int w, int sh) {
  constexpr int NJ = (NQ == 0) ? 7 : 6;
  constexpr int NZ2 = (NQ == 0) ? 3 : 2;
  constexpr int NZ1 = (NQ == 1) ? 3 : 2;
  constexpr int Z2I = (NQ == 0) ? 0 : (NQ == 1) ? 3 : (NQ == 2) ? 5 : 7;
  constexpr int Z1I = (NQ == 0) ? 0 : (NQ == 1) ? 2 : (NQ == 2) ? 5 : 7;
  constexpr int QI  = (NQ == 0) ? 0 : (NQ == 1) ? 2 : (NQ == 2) ? 3 : 5;
  constexpr int NS = 18;
  char* ws_dbg = ws;
  const int tid = threadIdx.x, l = tid & 63, g = l >> 4, c16 = l & 15;
  const int qstart = 108 * cg - ((cg & 1) ? 4 : 0);

  const int boff = (64 * sh + c16) * 64 + ((g ^ ((-(c16 >> 2)) & 3)) * 16);
  const int rowp = 16 * (g >> 1) + 4 * (g & 1) + (c16 >> 2);
  const int aoffz = 8192 + rowp * 288 + 8 * (c16 & 3);
  const int aoffq = 8192 + 18432 + rowp * 224 + 8 * (c16 & 3);
  auto rda = [&](const char* slot, int j, int h) -> bf16x4 {
    if (j < NZ2) return lds_tr(slot + aoffz + (Z2I + j) * 32 + h * 8 * 288);
    if (j < NZ2 + NZ1) return lds_tr(slot + aoffz + 9216 + (Z1I + j - NZ2) * 32 + h * 8 * 288);
    return lds_tr(slot + aoffq + (QI + j - NZ2 - NZ1) * 32 + h * 8 * 224);
  };
  auto rd = [&](Frag1<NJ>& f, int t) {
    const char* slot = lds + (t & 3) * K1_SLOT;
#pragma unroll
    for (int m = 0; m < 4; m++) f.b[m] = lds_b128(slot + boff + m * 1024);
#pragma unroll
    for (int j = 0; j < NJ; j++) f.a[j] = cat8(rda(slot, j, 0), rda(slot, j, 1));
  };
  f32x4 acc[NJ][4];
#pragma unroll
  for (int j = 0; j < NJ; j++)
#pragma unroll
    for (int m = 0; m < 4; m++) acc[j][m] = (f32x4){0.f, 0.f, 0.f, 0.f};
  auto step = [&](const Frag1<NJ>& cur, Frag1<NJ>& nxt, int t, bool doissue) {
    const char* slot = lds + ((t + 1) & 3) * K1_SLOT;
    bf16x4 lo[NJ];
#pragma unroll
    for (int x = 0; x < 4 * NJ; x++) {
      const int j = x >> 2, m = x & 3;
      acc[j][m] = mfma(cur.a[j], cur.b[m], acc[j][m]);
      if (x < 4) nxt.b[x] = lds_b128(slot + boff + x * 1024);
      else if (x < 4 + 2 * NJ) {
        const int y = x - 4, jj = y >> 1;
        if ((y & 1) == 0) lo[jj] = rda(slot, jj, 0);
        else nxt.a[jj] = cat8(lo[jj], rda(slot, jj, 1));
      }
      if (doissue && (x & 3) == 1 && (x >> 2) < 5) gs.piece(t + 4, x >> 2, w);
      SB();
    }
  };
  Frag1<NJ> f0, f1;
  wait_vm_lgkm0<15>();
  bar();
  TS(1, 1);
  rd(f0, 0);
#pragma unroll 1
  for (int t = 0; t < NS - 4; t += 2) {
    wait_vm_lgkm0<10>(); bar(); SB();
    step(f0, f1, t, true);
    wait_vm_lgkm0<10>(); bar(); SB();
    step(f1, f0, t + 1, true);
  }
  wait_vm_lgkm0<10>(); bar(); SB(); step(f0, f1, NS - 4, false);
  wait_vm_lgkm0<5>();  bar(); SB(); step(f1, f0, NS - 3, false);
  wait_vm_lgkm0<0>();  bar(); SB(); step(f0, f1, NS - 2, false);
#pragma unroll
  for (int j = 0; j < NJ; j++)
#pragma unroll
    for (int m = 0; m < 4; m++) acc[j][m] = mfma(f1.a[j], f1.b[m], acc[j][m]);
  TS(1, 2);
  if (w < 4) touch_s((w * 64 + l) * 128, w_sp + (size_t)blockIdx.x * 32768, (unsigned)(uintptr_t)(AS3 char*)lds + K1_PF + w * 256);
  {
    f32x2* st = (f32x2*)(lds + K1_STAT) + NQ * 128 + 64 * sh + c16;
#pragma unroll
    for (int m = 0; m < 4; m++) {
      f32x2 a2 = {0.f, 0.f}, q2 = a2;
#pragma unroll
      for (int j = 0; j < NZ2; j++) {
        const f32x2 u = gelu2(lo2(acc[j][m])), v = gelu2(hi2(acc[j][m]));
        acc[j][m] = __builtin_shufflevector(u, v, 0, 1, 2, 3);
        a2 += u + v;
        q2 = __builtin_elementwise_fma(u, u, __builtin_elementwise_fma(v, v, q2));
      }
      float a = a2[0] + a2[1], q = q2[0] + q2[1];
      a += __shfl_xor(a, 16); q += __shfl_xor(q, 16);
      a += __shfl_xor(a, 32); q += __shfl_xor(q, 32);
      if (g == 0) st[16 * m] = (f32x2){a, q};
    }
  }
  lds_bar();
  AS1 unsigned long long* gstat = (AS1 unsigned long long*)(ws + WS_STAT) + (size_t)rb * 16 * 128;
  if (tid < 128) {
    const f32x2* st = (const f32x2*)(lds + K1_STAT) + tid;
    f32x2 v = st[0] + st[128] + st[256] + st[384];
    const float v0 = v[0], v1 = v[1];
    unsigned lo = __builtin_bit_cast(unsigned, v0), hi = __builtin_bit_cast(unsigned, v1);
    if (hi == 0xffffffffu) hi = 0x7fc00000u;
    __hip_atomic_store(gstat + cg * 128 + tid, ((unsigned long long)hi << 32) | lo, __ATOMIC_RELAXED,
                       __HIP_MEMORY_SCOPE_AGENT);
  }
#pragma unroll
  for (int j = NZ2 + NZ1; j < NJ; j++) {
#pragma unroll
    for (int p = 0; p < 2; p++) {
      const int c = (QI + (j - NZ2 - NZ1)) * 16 + 4 * g + 2 * p + qstart - 108 * cg;
      const int hp = (c * 3641) >> 16, d = c - hp * 18;
      char* dst = lds + K1_QIMG + hp * 4608 + (64 * sh + c16) * 36 + d * 2;
      if (c >= 0 && c < 108) {
#pragma unroll
        for (int m = 0; m < 4; m++) *(unsigned*)(dst + m * 16 * 36) = pk2(acc[j][m][2 * p], acc[j][m][2 * p + 1]);
      }
    }
  }
#pragma unroll
  for (int j = 0; j < NZ1; j++)
#pragma unroll
    for (int mp = 0; mp < 2; mp++) {
      const f32x4 u = acc[NZ2 + j][2 * mp], v = acc[NZ2 + j][2 * mp + 1];
      const u32x4 o = {pk2v(gelu2(lo2(u))), pk2v(gelu2(hi2(u))), pk2v(gelu2(lo2(v))), pk2v(gelu2(hi2(v)))};
      st16(ws + WS_Z1 + ((size_t)(4 * rb + 2 * sh + mp) * 144 + 9 * cg + Z1I + j) * 1024 + l * 16, o);
    }
  const int prow = tid & 127, part = tid >> 7;
  unsigned long long sv[4];
#pragma unroll
  for (int i = 0; i < 4; i++)
    sv[i] = __hip_atomic_load(gstat + (part * 4 + i) * 128 + prow, __ATOMIC_RELAXED, __HIP_MEMORY_SCOPE_AGENT);
  lds_bar();
  TS(1, 3);
#pragma unroll
  for (int u = 0; u < 4; u++) {
    const int q = tid + 512 * u;
    if (u < 3 || q < 1728) {
      const int pi = (q * 3641) >> 20, rem = q - pi * 288;
      st16(ws + WS_QKV + ((size_t)(6 * cg + pi) * S_ + rb * 128) * 36 + rem * 16, *(const u32x4*)(lds + K1_QIMG + q * 16));
    }
  }
  {
    float a = 0.f, q = 0.f;
#pragma unroll
    for (int i = 0; i < 4; i++) {
      unsigned long long v = sv[i];
      while ((unsigned)(v >> 32) == 0xffffffffu)
        v = __hip_atomic_load(gstat + (part * 4 + i) * 128 + prow, __ATOMIC_RELAXED, __HIP_MEMORY_SCOPE_AGENT);
      a += __builtin_bit_cast(float, (unsigned)v);
      q += __builtin_bit_cast(float, (unsigned)(v >> 32));
    }
    ((f32x2*)(lds + K1_STAT2))[part * 128 + prow] = (f32x2){a, q};
  }
  lds_bar();
#pragma unroll
  for (int m = 0; m < 4; m++) {
    const f32x2* st = (const f32x2*)(lds + K1_STAT2) + 64 * sh + 16 * m + c16;
    f32x2 v = st[0] + st[128] + st[256] + st[384];
    const float mean = v[0] * (1.0f / FH);
    const float var = fmaxf(v[1] * (1.0f / FH) - mean * mean, 0.f);
    const float rs = 1.0f / __builtin_sqrtf(var + 1e-5f);
    const f32x2 mean2 = {mean, mean}, rs2 = {rs, rs};
    char* dst = lds + K1_ZIMG + (64 * sh + 16 * m + swap23(c16)) * 288 + Z2I * 32 + g * 8;
#pragma unroll
    for (int j = 0; j < NZ2; j++)
      *(u32x2*)(dst + j * 32) = (u32x2){pk2v((lo2(acc[j][m]) - mean2) * rs2), pk2v((hi2(acc[j][m]) - mean2) * rs2)};
  }
  lds_bar();
  {
    char* zt = ws + WS_Z2 + (size_t)cg * 589824 + (size_t)rb * 36864;
#pragma unroll
    for (int u = 0; u < 5; u++) {
      const int q = tid + 512 * u;
      if (u < 4 || q < 2304) *(u32x4*)(zt + q * 16) = *(const u32x4*)(lds + K1_ZIMG + q * 16);
    }
  }
  TS(1, 4);
}

__global__ __launch_bounds__(512, 2) void k1_proj(const char* __restrict__ w_in, const char* __restrict__ wqkv,
                                                  char* __restrict__ ws, const char* __restrict__ w_sp) {
  extern __shared__ __attribute__((aligned(16))) char lds[];
  const int b = blockIdx.x, xcd = b & 7, i = b >> 3;
  const int rb = 8 * (xcd & 1) + (i & 7), cg = 4 * (xcd >> 1) + (i >> 3);
  const int w = __builtin_amdgcn_readfirstlane(threadIdx.x >> 6);
  const int sh = w >> 2, nq = sh ? 3 - (w & 3) : (w & 3);
  char* ws_dbg = ws; TS(1, 0);
  K1Stage gs;
  gs.init(lds, w_in, wqkv, ws, rb, cg, w);
  gs.issue(0, w); gs.issue(1, w); gs.issue(2, w); gs.issue(3, w);
  if (nq == 0) k1_body<0>(lds, gs, ws, w_sp, rb, cg, w, sh);
  else if (nq == 1) k1_body<1>(lds, gs, ws, w_sp, rb, cg, w, sh);
  else if (nq == 2) k1_body<2>(lds, gs, ws, w_sp, rb, cg, w, sh);
  else k1_body<3>(lds, gs, ws, w_sp, rb, cg, w, sh);
}

struct AttnIn { u32x4 qv[2], k0, k1, v0, v1; unsigned k2, v2; };
DEVI void attn_load(AttnIn& in, const char* __restrict__ ws, int i) {
  const int b = blockIdx.x, h = b & 31, q0 = (b >> 5) * 256;
  const int tid = threadIdx.x, g = (tid & 63) >> 4, c16 = tid & 15;
  const char* qkv = ws + WS_QKV;
  const char* qp = qkv + ((size_t)h * S_ + q0 + 32 * (tid >> 6) + c16) * 36 + (g < 2 ? 16 * g : 20);
  const char* kp = qkv + ((size_t)(32 + h) * S_ + max(q0 - 256 + tid, 0)) * 36;
  const char* vp = kp + (size_t)32 * S_ * 36;
  if (i == 0) in.k0 = *(const u32x4_a4*)kp;
  if (i == 1) in.k1 = *(const u32x4_a4*)(kp + 16);
  if (i == 2) in.k2 = *(const unsigned*)(kp + 32);
  if (i == 3) in.v0 = *(const u32x4_a4*)vp;
  if (i == 4) in.v1 = *(const u32x4_a4*)(vp + 16);
  if (i == 5) in.v2 = *(const unsigned*)(vp + 32);
  if (i == 6) in.qv[0] = *(const u32x4_a4*)qp;
  if (i == 7) in.qv[1] = *(const u32x4_a4*)(qp + 16 * 36);
}

DEVI void attn_image(char* lds, const AttnIn& in) {
  const int tid = threadIdx.x, key = (blockIdx.x >> 5) * 256 - 256 + tid;
  const u32x4 k0 = in.k0, k1 = in.k1, v0 = in.v0, v1 = in.v1;
  const unsigned k2 = in.k2, v2 = in.v2;
  {
    const int f = (-(tid >> 2)) & 3;
    char* kr = lds + tid * 64;
    *(u32x4*)(kr + ((0 ^ f) * 16)) = k0;
    *(u32x4*)(kr + ((1 ^ f) * 16)) = k1;
    *(u32x4*)(kr + ((2 ^ f) * 16)) = (u32x4){k2, key < 0 ? 0x0000f149u : 0u, 0, 0};
    *(u32x4*)(kr + ((3 ^ f) * 16)) = (u32x4){0, 0, 0, 0};
    char* vr = lds + 32768 + tid * 64;
    const int x = ((tid >> 2) & 1) * 32;
    *(u32x4*)(vr + (0 ^ x)) = v0;
    *(u32x4*)(vr + (16 ^ x)) = v1;
    *(u32x4*)(vr + (32 ^ x)) = (u32x4){v2, 0x00003f80u, 0, 0};
    *(u32x4*)(vr + (48 ^ x)) = (u32x4){0, 0, 0, 0};
  }
}

DEVI void attn_phase(char* lds, const AttnIn& in, const char* __restrict__ wo, const char* __restrict__ w_out,
                     char* __restrict__ ws) {
  const int b = blockIdx.x, h = b & 31, qb = b >> 5;
  const int tid = threadIdx.x, l = tid & 63, g = l >> 4, c16 = l & 15;
  const int w = __builtin_amdgcn_readfirstlane(tid >> 6);
  char* ws_dbg = ws;
  bf16x8 qf[2];
#pragma unroll
  for (int m = 0; m < 2; m++) {
    u32x4 v = in.qv[m];
    if (g >= 2) v = (u32x4){g == 2 ? v[3] : 0u, g == 2 ? 0x00003f80u : 0u, 0, 0};
    qf[m] = __builtin_bit_cast(bf16x8, v);
  }
  TW(2, 0);
  const int wu = w * 256 + b;
  WPrep wp;
  if (wu < 810) wp.load(wo, w_out, wu / 9, wu % 9, l);
  const float sc = 0.23570226039551584f * 1.4426950408889634f;
  const int koff = (32 * w + c16) * 64 + ((g ^ ((-(c16 >> 2)) & 3)) * 16);
  const int voff = 32768 + (32 * w + 4 * g + (c16 >> 2)) * 64 + 8 * (c16 & 3);
  const int vx = (g & 1) * 32;
#pragma unroll
  for (int m = 0; m < 2; m++) {
    f32x4 s[17];
#pragma unroll
    for (int i = 0; i < 17; i++) {
      bf16x8 kf = lds_b128(lds + koff + (i + m) * 1024);
      s[i] = mfma(kf, qf[m], (f32x4){0.f, 0.f, 0.f, 0.f});
    }
    const float ninf = -__builtin_inff();
#pragma unroll
    for (int r = 0; r < 4; r++) {
      if (!(c16 < 4 * g + r)) s[0][r] = ninf;
      if (!(4 * g + r <= c16)) s[16][r] = ninf;
    }
    float mxa = ninf, mxb = ninf;
#pragma unroll
    for (int i = 0; i < 17; i++) { mxa = fmx(fmx(mxa, s[i][0]), s[i][1]); mxb = fmx(fmx(mxb, s[i][2]), s[i][3]); }
    float mx = fmx(mxa, mxb);
    mx = fmx(mx, __shfl_xor(mx, 16));
    mx = fmx(mx, __shfl_xor(mx, 32));
    const float nb = -mx * sc;
    const f32x2 sc2 = {sc, sc}, nb2 = {nb, nb};
    unsigned p[18][2];
#pragma unroll
    for (int i = 0; i < 17; i++) {
      const f32x2 ta = __builtin_elementwise_fma((f32x2){s[i][0], s[i][1]}, sc2, nb2);
      const f32x2 tb = __builtin_elementwise_fma((f32x2){s[i][2], s[i][3]}, sc2, nb2);
      const float a0 = ta[0], a1 = ta[1], b0 = tb[0], b1 = tb[1];
      p[i][0] = pk2(__builtin_amdgcn_exp2f(a0), __builtin_amdgcn_exp2f(a1));
      p[i][1] = pk2(__builtin_amdgcn_exp2f(b0), __builtin_amdgcn_exp2f(b1));
    }
    p[17][0] = 0; p[17][1] = 0;
    f32x4 o0 = {0.f, 0.f, 0.f, 0.f}, o1 = o0;
#pragma unroll
    for (int i = 0; i < 9; i++) {
      const int r0 = (2 * i + m) * 1024, r1 = (i < 8) ? r0 + 1024 : r0;
      bf16x8 pf = __builtin_bit_cast(bf16x8, (u32x4){p[2 * i][0], p[2 * i][1], p[2 * i + 1][0], p[2 * i + 1][1]});
      bf16x8 va = cat8(lds_tr(lds + voff + vx + r0), lds_tr(lds + voff + vx + r1));
      bf16x8 vb = cat8(lds_tr(lds + voff + (32 ^ vx) + r0), lds_tr(lds + voff + (32 ^ vx) + r1));
      o0 = mfma(va, pf, o0);
      o1 = mfma(vb, pf, o1);
    }
    const float den = __shfl(o1[2], c16);
    const float inv = 1.0f / den;
    char* op = ws + WS_O + (size_t)(16 * qb + 2 * w + m) * 18432;
    *(u32x2*)(op + (h >> 1) * 1024 + l * 16 + (h & 1) * 8) = (u32x2){pk2(o0[0] * inv, o0[1] * inv), pk2(o0[2] * inv, o0[3] * inv)};
    if (g == 0)
      *(unsigned*)(op + (16 + (h >> 4)) * 1024 + (((h & 7) >> 1) * 16 + c16) * 16 + ((h >> 3) & 1) * 8 + (h & 1) * 4) =
          pk2(o1[0] * inv, o1[1] * inv);
  }
  if (wu < 810) wp.store(ws, wu / 9, wu % 9, l);
  TW(2, 1);
}

constexpr int G_SLOT = 16384 + 18432;
constexpr int G_LDS = 4 * G_SLOT;
constexpr int K2_LDS = 81920 + 65536;
template <int NJ> struct Frag2 { bf16x8 a[NJ]; bf16x8 b[4]; };

struct GStage {
  unsigned bo[2], ao[3], bdst, adst;
  const char* Bb;
  const char* Ab;
  DEVI void init(char* lds, const char* B, const char* A, int w) {
    const int l = threadIdx.x & 63;
    Bb = B; Ab = A;
#pragma unroll
    for (int i = 0; i < 2; i++) {
      int r = (2 * w + i) * 8 + (l >> 3);
      int c = (l & 7) ^ ((r >> 1) & 7);
      bo[i] = r * 4096 + c * 16;
    }
#pragma unroll
    for (int i = 0; i < 3; i++) {
      int q = w * 144 + i * 64 + ((i == 2) ? (l & 15) : l);
      ao[i] = q * 16;
    }
    const unsigned lb = (unsigned)(uintptr_t)(AS3 char*)lds;
    bdst = lb + w * 2048; adst = lb + 16384 + w * 2304;
  }
  DEVI void piece(int t, int i) const {
    const unsigned so = (t & 3) * G_SLOT;
    const size_t astep = 18432;
    if (i < 2) glds_s(bo[i], Bb + t * 128, bdst + so + i * 1024);
    else if (i < 4) glds_s(ao[i - 2], Ab + (size_t)t * astep, adst + so + (i - 2) * 1024);
    else glds_s16(ao[2], Ab + (size_t)t * astep, adst + so + 2048);
  }
  DEVI void issue(int t) const {
#pragma unroll
    for (int i = 0; i < 5; i++) piece(t, i);
  }
};

template <int NJ, int NS, int NH, class Hook>
DEVI void gemm_core(char* lds, const GStage& gs, int w, f32x4 (&acc)[NJ][4], char* ws_dbg, int kid, Hook hook) {
  const int l = threadIdx.x & 63, g = l >> 4, c16 = l & 15;
  const int fh = w >> 2, th = w & 1, kh = (w >> 1) & 1;
  const int boff = (64 * th + c16) * 128 + (((4 * kh + g) ^ (c16 >> 1)) * 16);
  const int aoff = 16384 + (32 * kh + 16 * (g >> 1) + 4 * (g & 1) + (c16 >> 2)) * 288 + fh * 160 + 8 * (c16 & 3);
  auto rd = [&](Frag2<NJ>& f, int t) {
    const char* slot = lds + (t & 3) * G_SLOT;
#pragma unroll
    for (int m = 0; m < 4; m++) f.b[m] = lds_b128(slot + boff + m * 2048);
#pragma unroll
    for (int j = 0; j < NJ; j++)
      f.a[j] = cat8(lds_tr(slot + aoff + j * 32), lds_tr(slot + aoff + j * 32 + 8 * 288));
  };
  auto step = [&](const Frag2<NJ>& cur, Frag2<NJ>& nxt, int t, bool doissue, int hb = -1) {
    const char* slot = lds + ((t + 1) & 3) * G_SLOT;
    const char* pb = slot + boff;
    const char* pa = slot + aoff;
    bf16x4 lo[NJ];
#pragma unroll
    for (int x = 0; x < 4 * NJ; x++) {
      const int j = x >> 2, m = x & 3;
      acc[j][m] = mfma(cur.a[j], cur.b[m], acc[j][m]);
      if (x < 4) nxt.b[x] = lds_b128(pb + x * 2048);
      else if (x < 4 + 2 * NJ) {
        const int y = x - 4, jj = y >> 1;
        if ((y & 1) == 0) lo[jj] = lds_tr(pa + jj * 32);
        else nxt.a[jj] = cat8(lo[jj], lds_tr(pa + jj * 32 + 8 * 288));
      }
      if (doissue && (x % 3) == 1 && x / 3 < 5) gs.piece(t + 4, x / 3);
      if (hb >= 0 && (x % 3) == 1 && x / 3 < 5 && hb + x / 3 < NH) hook(hb + x / 3);
      SB();
    }
  };
  auto mm = [&](const Frag2<NJ>& f) {
#pragma unroll
    for (int j = 0; j < NJ; j++)
#pragma unroll
      for (int m = 0; m < 4; m++) acc[j][m] = mfma(f.a[j], f.b[m], acc[j][m]);
  };
#pragma unroll
  for (int j = 0; j < NJ; j++)
#pragma unroll
    for (int m = 0; m < 4; m++) acc[j][m] = (f32x4){0.f, 0.f, 0.f, 0.f};
  Frag2<NJ> f0, f1;
  wait_vm_lgkm0<15>();
  bar();
  TS(kid, 1);
  rd(f0, 0);
  constexpr int M = NS - 4, MP = M & ~1;
#pragma unroll 1
  for (int t = 0; t < MP; t += 2) {
    wait_vm_lgkm0<10>(); bar(); SB();
    step(f0, f1, t, true);
    wait_vm_lgkm0<10>(); bar(); SB();
    step(f1, f0, t + 1, true);
  }
  static_assert((M & 1) == 0, "even step count");
  static_assert(NH >= 10 && NH <= 15, "hook pieces span three steps");
  wait_vm_lgkm0<10>(); bar(); SB(); step(f0, f1, NS - 4, false, 0);
  wait_vm_lgkm0<10>(); bar(); SB(); step(f1, f0, NS - 3, false, 5);
  wait_vm_lgkm0<10>(); bar(); SB(); step(f0, f1, NS - 2, false, 10);
  mm(f1);
  TS(kid, 2);
}

template <int NJ>
DEVI void khalf_exchange(char* lds, int w, f32x4 (&acc)[NJ][4]) {
  const int l = threadIdx.x & 63, kh = (w >> 1) & 1;
  lds_bar();
  f32x4* mine = (f32x4*)(lds + w * 10240) + l;
  const f32x4* other = (const f32x4*)(lds + (w ^ 2) * 10240) + l;
  if (kh == 0) {
#pragma unroll
    for (int j = 0; j < NJ; j++) { mine[(2 * j) * 64] = acc[j][2]; mine[(2 * j + 1) * 64] = acc[j][3]; }
  } else {
#pragma unroll
    for (int j = 0; j < NJ; j++) {
      mine[(2 * j) * 64] = acc[j][0]; mine[(2 * j + 1) * 64] = acc[j][1];
      acc[j][0] = acc[j][2]; acc[j][1] = acc[j][3];
    }
  }
  lds_bar();
#pragma unroll
  for (int j = 0; j < NJ; j++) { acc[j][0] += other[(2 * j) * 64]; acc[j][1] += other[(2 * j + 1) * 64]; }
}

template <int NJ>
DEVI void k2_body(char* lds, const GStage& gs, const char* __restrict__ b_sp, char* __restrict__ ws,
                  int tb, int fb, int w, AttnIn& ain) {
  f32x4 acc[NJ][4];
  const int l = threadIdx.x & 63, c16 = l & 15;
  const int fh = w >> 2, th = w & 1, kh = (w >> 1) & 1;
  const int trow = tb * 128 + 64 * th + 32 * kh + c16;
  u32x4 z[NJ];
  unsigned bsr[2];
  gemm_core<NJ, 32, 10 + NJ>(lds, gs, w, acc, ws, 2, [&](int i) {
    if (i < 8) attn_load(ain, ws, i);
    else if (i < 10) bsr[i - 8] = *(const unsigned short*)(b_sp + (trow + 16 * (i - 8)) * 2);
    else z[i - 10] = *(const u32x4*)(ws + WS_Z1 + ((size_t)(4 * tb + 2 * th + kh) * 144 + 9 * fb + 5 * fh + (i - 10)) * 1024 + l * 16);
  });
  khalf_exchange<NJ>(lds, w, acc);
  const float bs[2] = {__builtin_bit_cast(float, bsr[0] << 16), __builtin_bit_cast(float, bsr[1] << 16)};
  { char* ws_dbg = ws; TS(2, 3); }
  const int J0 = 9 * fb + 5 * fh;
#pragma unroll
  for (int m = 0; m < 2; m++) {
    u32x2 o[NJ];
#pragma unroll
    for (int j = 0; j < NJ; j++) {
      const f32x4 a = acc[j][m];
      const unsigned z0 = z[j][2 * m], z1 = z[j][2 * m + 1];
      o[j] = (u32x2){pk2(bflo(z0) * (a[0] + bs[m]), bfhi(z0) * (a[1] + bs[m])),
                     pk2(bflo(z1) * (a[2] + bs[m]), bfhi(z1) * (a[3] + bs[m]))};
    }
    char* hp = ws + WS_H + (size_t)(8 * tb + 4 * th + 2 * kh + m) * 73728 + (J0 >> 1) * 1024 + l * 16;
    if (J0 & 1) {
      *(u32x2*)(hp + 8) = o[0];
      st16(hp + 1024, (u32x4){o[1][0], o[1][1], o[2][0], o[2][1]});
      if constexpr (NJ == 5) st16(hp + 2048, (u32x4){o[3][0], o[3][1], o[4][0], o[4][1]});
      else *(u32x2*)(hp + 2048) = o[3];
    } else {
      st16(hp, (u32x4){o[0][0], o[0][1], o[1][0], o[1][1]});
      st16(hp + 1024, (u32x4){o[2][0], o[2][1], o[3][0], o[3][1]});
      if constexpr (NJ == 5) *(u32x2*)(hp + 2048) = o[4];
    }
  }
}
__global__ __launch_bounds__(512, 2) void k2_sgu(const char* __restrict__ w_sp, const char* __restrict__ b_sp,
                                                 const char* __restrict__ wo, const char* __restrict__ w_out,
                                                 char* __restrict__ ws) {
  extern __shared__ __attribute__((aligned(16))) char lds[];
  const int b = blockIdx.x, xcd = b & 7, i = b >> 3;
  const int tb = 8 * (xcd & 1) + (i & 7), fb = 4 * (xcd >> 1) + (i >> 3);
  const int w = __builtin_amdgcn_readfirstlane(threadIdx.x >> 6);
  char* ws_dbg = ws; TS(2, 0);
  GStage gs;
  gs.init(lds, w_sp + (size_t)tb * 128 * 4096, ws + WS_Z2 + (size_t)fb * 589824, w);
  gs.issue(0); gs.issue(1); gs.issue(2); gs.issue(3);
  AttnIn ain;
  if (w < 4) k2_body<5>(lds, gs, b_sp, ws, tb, fb, w, ain);
  else k2_body<4>(lds, gs, b_sp, ws, tb, fb, w, ain);
  TS(2, 4);
  attn_image(lds + 81920, ain);
  lds_bar();
  attn_phase(lds + 81920, ain, wo, w_out, ws);
}

constexpr int KC_LDS = 8 * 20 * 1024;
struct FragC { u32x4 h[5]; u32x4 wv[4]; };

template <int NB, int KSTEP>
DEVI void kc_stream(const char* __restrict__ hb, const unsigned (&ho)[5], const char* __restrict__ wb, unsigned wofs,
                    f32x4 (&acc)[5][4]) {
  constexpr int NBUF = 3;
  auto ld = [&](FragC& f, int i) {
    const int ii = i;
#pragma unroll
    for (int m = 0; m < 5; m++) f.h[m] = *(const u32x4*)(hb + (size_t)ii * (KSTEP * 1024) + ho[m]);
#pragma unroll
    for (int j = 0; j < 4; j++) f.wv[j] = *(const u32x4*)(wb + (size_t)ii * (KSTEP * 36 * 1024) + wofs + j * 1024);
    SB();
  };
  auto mm = [&](const FragC& f) {
#pragma unroll
    for (int m = 0; m < 5; m++)
#pragma unroll
      for (int j = 0; j < 4; j++)
        acc[m][j] = mfma(__builtin_bit_cast(bf16x8, f.wv[j]), __builtin_bit_cast(bf16x8, f.h[m]), acc[m][j]);
    SB();
  };
  FragC f[NBUF];
#pragma unroll
  for (int i = 0; i < NBUF - 1; i++) ld(f[i], i);
#pragma unroll
  for (int i = 0; i < NB; i++) {
    if (i + NBUF - 1 < NB) ld(f[(i + NBUF - 1) % NBUF], i + NBUF - 1);
    mm(f[i % NBUF]);
  }
}

__global__ __launch_bounds__(512, 2) void kc_out(const char* __restrict__ x, char* __restrict__ ws,
                                                 char* __restrict__ out) {
  extern __shared__ __attribute__((aligned(16))) char lds[];
  const int b = blockIdx.x, xcd = b & 7, i = b >> 3;
  const int unit = xcd * 30 + i;
  if (unit >= 234) return;
  const int tb = unit / 9, db = unit - tb * 9;
  const int l = threadIdx.x & 63, g = l >> 4, c16 = l & 15;
  const int w = __builtin_amdgcn_readfirstlane(threadIdx.x >> 6);
  char* ws_dbg = ws; TS(3, 0); TS(3, 1);
  f32x4 acc[5][4];
#pragma unroll
  for (int m = 0; m < 5; m++)
#pragma unroll
    for (int j = 0; j < 4; j++) acc[m][j] = (f32x4){0.f, 0.f, 0.f, 0.f};
  unsigned ho[5];
  if (w < 6) {
#pragma unroll
    for (int m = 0; m < 5; m++) ho[m] = min(5 * tb + m, 127) * 73728 + l * 16;
    kc_stream<12, 6>(ws + WS_H + w * 1024, ho, ws + WS_WF + (size_t)(w * 36 + db * 4) * 1024, l * 16, acc);
  } else {
#pragma unroll
    for (int m = 0; m < 5; m++) ho[m] = min(5 * tb + m, 127) * 18432 + l * 16;
    kc_stream<9, 2>(ws + WS_O + (w - 6) * 1024, ho, ws + WS_WF + (size_t)((72 + w - 6) * 36 + db * 4) * 1024, l * 16, acc);
  }
  TS(3, 2); TW(3, 0);
  u32x2 xv[3];
#pragma unroll
  for (int q = 0; q < 3; q++) {
    const int st = min(w + 8 * q, 19), ts = min(5 * tb + (st >> 2), 127);
    xv[q] = *(const u32x2*)(x + (size_t)(ts * 16 + c16) * 1152 + (db * 64 + (st & 3) * 16 + 4 * g) * 2);
  }
  f32x4* red = (f32x4*)lds;
#pragma unroll
  for (int m = 0; m < 5; m++)
#pragma unroll
    for (int j = 0; j < 4; j++) red[(w * 20 + m * 4 + j) * 64 + l] = acc[m][j];
  asm volatile("s_waitcnt lgkmcnt(0)" ::: "memory");
  TW(3, 1);
  lds_bar();
  TS(3, 3);
#pragma unroll
  for (int q = 0; q < 3; q++) {
    const int st = w + 8 * q, ts = 5 * tb + (st >> 2);
    if (st < 20 && ts < 128) {
      const f32x4* p = red + st * 64 + l;
      f32x4 y = (p[0] + p[20 * 64]) + (p[40 * 64] + p[60 * 64]) + (p[80 * 64] + p[100 * 64]);
      f32x4 sv = p[120 * 64] + p[140 * 64];
      const float xf[4] = {bflo(xv[q][0]), bfhi(xv[q][0]), bflo(xv[q][1]), bfhi(xv[q][1])};
      float o[4];
#pragma unroll
      for (int e = 0; e < 4; e++) {
        const float sig = __builtin_amdgcn_rcpf(1.0f + __builtin_amdgcn_exp2f(-1.4426950408889634f * y[e]));
        o[e] = xf[e] + sv[e] * (y[e] * sig);
      }
      *(u32x2*)(out + (size_t)(ts * 16 + c16) * 1152 + (db * 64 + (st & 3) * 16 + 4 * g) * 2) =
          (u32x2){pk2(o[0], o[1]), pk2(o[2], o[3])};
    }
  }
  TS(3, 4);
}

extern "C" void kernel_launch(void* const* d_in, const int* in_sizes, int n_in, void* d_out, int out_size,
                              void* d_ws, size_t ws_size, hipStream_t stream) {
  const char* x = (const char*)d_in[0];
  const char* wqkv = (const char*)d_in[1];
  const char* wo = (const char*)d_in[2];
  const char* w_in = (const char*)d_in[4];
  const char* w_sp = (const char*)d_in[5];
  const char* b_sp = (const char*)d_in[6];
  const char* w_out = (const char*)d_in[7];
  char* ws = (char*)d_ws;
  (void)hipFuncSetAttribute((const void*)k1_proj, hipFuncAttributeMaxDynamicSharedMemorySize, K1_LDS);
  (void)hipFuncSetAttribute((const void*)k2_sgu, hipFuncAttributeMaxDynamicSharedMemorySize, K2_LDS);
  (void)hipFuncSetAttribute((const void*)kc_out, hipFuncAttributeMaxDynamicSharedMemorySize, KC_LDS);
  constexpr int NPF = 57344 / 256;
  k0_ln<<<256 + NPF, 64, 0, stream>>>(x, ws, w_in, wqkv);
  k1_proj<<<256, 512, K1_LDS, stream>>>(w_in, wqkv, ws, w_sp);
  k2_sgu<<<256, 512, K2_LDS, stream>>>(w_sp, b_sp, wo, w_out, ws);
  kc_out<<<8 * 30, 512, KC_LDS, stream>>>(x, ws, (char*)d_out);
}
```

```cpp
#include <hip/hip_runtime.h>
#include <hip/hip_bf16.h>
#include <stdint.h>
#pragma clang diagnostic ignored "-Winline-asm"

typedef __attribute__((ext_vector_type(8))) __bf16 bf16x8;
typedef __attribute__((ext_vector_type(4))) __bf16 bf16x4;
typedef __attribute__((ext_vector_type(2))) __bf16 bf16x2;
typedef __attribute__((ext_vector_type(4))) float f32x4;
typedef __attribute__((ext_vector_type(2))) float f32x2;
typedef __attribute__((ext_vector_type(4))) unsigned u32x4;
typedef __attribute__((ext_vector_type(2))) unsigned u32x2;
typedef u32x4 u32x4_a4 __attribute__((aligned(4)));
typedef u32x2 u32x2_a4 __attribute__((aligned(4)));
#define AS1 __attribute__((address_space(1)))
#define AS3 __attribute__((address_space(3)))
#define DEVI __device__ __forceinline__

constexpr int S_ = 2048, D_ = 576, FH = 2304;

constexpr size_t WS_XN   = 0;
constexpr size_t WS_Z1   = WS_XN + (size_t)S_ * D_ * 2;
constexpr size_t WS_Z2   = WS_Z1 + (size_t)S_ * FH * 2;
constexpr size_t WS_H    = WS_Z2 + (size_t)S_ * FH * 2;
constexpr size_t WS_QKV  = WS_H + (size_t)S_ * FH * 2;
constexpr size_t WS_O    = WS_QKV + (size_t)96 * S_ * 18 * 2;
constexpr size_t WS_STAT = WS_O + (size_t)S_ * D_ * 2 + (size_t)4 * S_ * D_ * 4;
constexpr size_t WS_WF   = WS_STAT + (size_t)16 * 16 * 128 * 8;

DEVI void glds16(const char* g, char* lds) {
  __builtin_amdgcn_global_load_lds((const AS1 void*)g, (AS3 void*)lds, 16, 0, 0);
}
DEVI void glds_s(unsigned voff, const char* sbase, unsigned ldsa) {
  asm volatile("s_mov_b32 m0, %2\n\ts_nop 0\n\tglobal_load_lds_dwordx4 %0, %1" ::"v"(voff), "s"(sbase), "s"(ldsa) : "memory", "m0");
}
DEVI void glds_s16(unsigned voff, const char* sbase, unsigned ldsa) {
  asm volatile("s_mov_b32 m0, %2\n\ts_mov_b64 exec, 0xffff\n\tglobal_load_lds_dwordx4 %0, %1\n\ts_mov_b64 exec, -1"
               ::"v"(voff), "s"(sbase), "s"(ldsa) : "memory", "m0");
}
DEVI void glds_s8(unsigned voff, const char* sbase, unsigned ldsa) {
  asm volatile("s_mov_b32 m0, %2\n\ts_mov_b64 exec, 0xff\n\tglobal_load_lds_dwordx4 %0, %1\n\ts_mov_b64 exec, -1"
               ::"v"(voff), "s"(sbase), "s"(ldsa) : "memory", "m0");
}
DEVI void touch_s(unsigned voff, const char* sbase, unsigned ldsa) {
  asm volatile("s_mov_b32 m0, %2\n\ts_nop 0\n\tglobal_load_lds_dword %0, %1" ::"v"(voff), "s"(sbase), "s"(ldsa) : "memory", "m0");
}
DEVI void st16(char* p, u32x4 v) { *(u32x4*)p = v; }
#define SB() __builtin_amdgcn_sched_barrier(0)
DEVI bf16x8 lds_b128(const char* p) { return *(const AS3 bf16x8*)p; }
DEVI bf16x4 lds_tr(const char* p) { return __builtin_amdgcn_ds_read_tr16_b64_v4bf16((AS3 bf16x4*)p); }
DEVI bf16x8 cat8(bf16x4 a, bf16x4 b) { return __builtin_shufflevector(a, b, 0, 1, 2, 3, 4, 5, 6, 7); }
DEVI f32x4 mfma(bf16x8 a, bf16x8 b, f32x4 c) { return __builtin_amdgcn_mfma_f32_16x16x32_bf16(a, b, c, 0, 0, 0); }
DEVI unsigned pk2(float a, float b) {
  f32x2 v = {a, b};
  bf16x2 r = __builtin_convertvector(v, bf16x2);
  return __builtin_bit_cast(unsigned, r);
}
DEVI float bflo(unsigned u) { return __builtin_bit_cast(float, u << 16); }
DEVI float bfhi(unsigned u) { return __builtin_bit_cast(float, u & 0xffff0000u); }
template <int N> DEVI void wait_vm_lgkm0() { asm volatile("s_waitcnt vmcnt(%0) lgkmcnt(0)" ::"n"(N) : "memory"); }
DEVI void bar() { __builtin_amdgcn_s_barrier(); asm volatile("" ::: "memory"); }
DEVI void lds_bar() { asm volatile("s_waitcnt lgkmcnt(0)" ::: "memory"); bar(); }
DEVI float fmx(float a, float b) { return __builtin_elementwise_maximum(a, b); }
DEVI int swap23(int r) { return (r & ~12) | ((r & 4) << 1) | ((r & 8) >> 1); }

struct WPrep {
  unsigned short v[4][8];
  DEVI void load(const char* __restrict__ wo, const char* __restrict__ w_out, int kb, int dq, int l) {
    const int g = l >> 4, c16 = l & 15;
#pragma unroll
    for (int e = 0; e < 8; e++) {
      const int r = e & 3, J = 2 * (kb < 72 ? kb : kb - 72) + (e >> 2);
      const char* src;
      if (kb < 72) src = w_out + (size_t)(16 * J + 4 * g + r) * 1152;
      else if (J < 32) src = wo + (size_t)(18 * J + 4 * g + r) * 1152;
      else src = wo + (size_t)((8 * (J - 32) + 2 * g + (r >> 1)) * 18 + 16 + (r & 1)) * 1152;
#pragma unroll
      for (int j = 0; j < 4; j++) v[j][e] = *(const unsigned short*)(src + (dq * 64 + j * 16 + c16) * 2);
    }
  }
  DEVI void store(char* __restrict__ ws, int kb, int dq, int l) const {
#pragma unroll
    for (int j = 0; j < 4; j++) {
      u32x4 o;
#pragma unroll
      for (int e = 0; e < 4; e++) o[e] = (unsigned)v[j][2 * e] | ((unsigned)v[j][2 * e + 1] << 16);
      *(u32x4*)(ws + WS_WF + ((size_t)kb * 36 + dq * 4 + j) * 1024 + l * 16) = o;
    }
  }
};

__global__ __launch_bounds__(64) void k0_ln(const char* __restrict__ x, char* __restrict__ ws,
                                            const char* __restrict__ w_in, const char* __restrict__ wqkv) {
  __shared__ __attribute__((aligned(16))) char pf[256];
  const int l = threadIdx.x, b = blockIdx.x;
  if (b >= 256) {
    const int u = b - 256;
    const unsigned lb = (unsigned)(uintptr_t)(AS3 char*)pf;
#pragma unroll
    for (int i = 0; i < 4; i++) {
      const int ln = (u * 4 + i) * 64;
      if (ln < 41472) touch_s(l * 128, w_in + (size_t)ln * 128, lb);
      else if (ln < 41472 + 15552) touch_s(l * 128, wqkv + (size_t)(ln - 41472) * 128, lb);
    }
    asm volatile("s_waitcnt vmcnt(0)" ::: "memory");
    return;
  }
  const int row = b * 8 + (l >> 3), c0 = l & 7;
  const char* src = x + (size_t)row * 1152 + c0 * 16;
  u32x4 v[9];
#pragma unroll
  for (int i = 0; i < 9; i++) v[i] = *(const u32x4*)(src + i * 128);
  *(u32x4*)(ws + WS_STAT + (size_t)b * 1024 + l * 16) = (u32x4){~0u, ~0u, ~0u, ~0u};
  float s = 0.f;
#pragma unroll
  for (int i = 0; i < 9; i++)
#pragma unroll
    for (int e = 0; e < 4; e++) s += bflo(v[i][e]) + bfhi(v[i][e]);
  s += __shfl_xor(s, 1); s += __shfl_xor(s, 2); s += __shfl_xor(s, 4);
  const float mean = s * (1.0f / 576.0f);
  float q = 0.f;
#pragma unroll
  for (int i = 0; i < 9; i++)
#pragma unroll
    for (int e = 0; e < 4; e++) {
      float a = bflo(v[i][e]) - mean, c = bfhi(v[i][e]) - mean;
      q += a * a + c * c;
    }
  q += __shfl_xor(q, 1); q += __shfl_xor(q, 2); q += __shfl_xor(q, 4);
  const float rs = 1.0f / __builtin_sqrtf(q * (1.0f / 576.0f) + 1e-5f);
  const int r = row & 127;
  char* dst = ws + WS_XN + (size_t)((row >> 7) * 18 + (c0 >> 2)) * 8192 + r * 64 + (((c0 & 3) ^ ((-(r >> 2)) & 3)) * 16);
#pragma unroll
  for (int i = 0; i < 9; i++) {
    u32x4 o;
#pragma unroll
    for (int e = 0; e < 4; e++) o[e] = pk2((bflo(v[i][e]) - mean) * rs, (bfhi(v[i][e]) - mean) * rs);
    *(u32x4*)(dst + i * 16384) = o;
  }
}

constexpr int K1_SLOT = 8192 + 25600;
constexpr int K1_STAT = 4 * K1_SLOT;
constexpr int K1_STAT2 = K1_STAT + 4096;
constexpr int K1_PF = K1_STAT2 + 4096;
constexpr int K1_LDS = K1_PF + 1024;
constexpr int K1_QIMG = 0;
constexpr int K1_ZIMG = 32768;

template <int NJ> struct Frag1 { bf16x8 a[NJ]; bf16x8 b[4]; };

struct K1Stage {
  unsigned xo, wo[4], xdst, wdst;
  const char* Xb;
  const char* Wa;
  const char* Wc;
  const char* Wq;
  unsigned cstep;
  DEVI void init(char* lds, const char* w_in, const char* wqkv, const char* ws, int rb, int cg, int w) {
    const int l = threadIdx.x & 63;
    const int qstart = 108 * cg - ((cg & 1) ? 4 : 0);
    Xb = ws + WS_XN + (size_t)rb * 18 * 8192;
    xo = w * 1024 + l * 16;
#pragma unroll
    for (int i = 0; i < 4; i++) {
      const int ins = (i < 3) ? w + 8 * i : 24;
      if (ins < 18) {
        const int ty = ins >= 9, q = (ins - 9 * ty) * 64 + l;
        const int rho = q / 18, c = q - rho * 18;
        wo[i] = swap23(rho) * 9216 + ((ty ? 0 : FH) + 144 * cg + c * 8) * 2;
      } else {
        const int q = (i < 3) ? (ins - 18) * 64 + l : 6 * 64 + w * 8 + (l & 7);
        const int rho = q / 14, c = q - rho * 14;
        wo[i] = swap23(rho) * 3456 + (qstart + c * 8) * 2;
      }
    }
    Wa = w_in; Wq = wqkv;
    Wc = (w < 2) ? w_in : wqkv;
    cstep = (w < 2) ? 32 * 9216 : 32 * 3456;
    const unsigned lb = (unsigned)(uintptr_t)(AS3 char*)lds;
    xdst = lb + w * 1024;
    wdst = lb + 8192 + w * 1024;
  }
  DEVI void piece(int t, int i, int w) const {
    const unsigned so = (t & 3) * K1_SLOT;
    if (i == 0) glds_s(xo, Xb + t * 8192, xdst + so);
    else if (i == 1) glds_s(wo[0], Wa + (size_t)t * (32 * 9216), wdst + so);
    else if (i == 2) glds_s(wo[1], Wa + (size_t)t * (32 * 9216), wdst + so + 8192);
    else if (i == 3) glds_s(wo[2], Wc + (size_t)t * cstep, wdst + so + 16384);
    else glds_s8(wo[3], Wq + (size_t)t * (32 * 3456), wdst + so + 24576 - w * 896);
  }
  DEVI void issue(int t, int w) const {
#pragma unroll
    for (int i = 0; i < 5; i++) piece(t, i, w);
  }
};

DEVI f32x2 gelu2(f32x2 y) {
  const f32x2 c1 = {-0.1029432397f, -0.1029432397f}, c0 = {-2.3022081983f, -2.3022081983f};
  const f32x2 a = y * __builtin_elementwise_fma(y * y, c1, c0);
  const float a0 = a[0], a1 = a[1];
  const f32x2 d = (f32x2){__builtin_amdgcn_exp2f(a0), __builtin_amdgcn_exp2f(a1)} + (f32x2){1.0f, 1.0f};
  const float d0 = d[0], d1 = d[1];
  return y * (f32x2){__builtin_amdgcn_rcpf(d0), __builtin_amdgcn_rcpf(d1)};
}
DEVI unsigned pk2v(f32x2 v) { return __builtin_bit_cast(unsigned, __builtin_convertvector(v, bf16x2)); }
DEVI f32x2 lo2(f32x4 v) { return __builtin_shufflevector(v, v, 0, 1); }
DEVI f32x2 hi2(f32x4 v) { return __builtin_shufflevector(v, v, 2, 3); }

template <int NQ>
DEVI void k1_body(char* lds, const K1Stage& gs, char* __restrict__ ws, const char* __restrict__ w_sp,
                  int rb, int cg, int w, int sh) {
  constexpr int NJ = (NQ == 0) ? 7 : 6;
  constexpr int NZ2 = (NQ == 0) ? 3 : 2;
  constexpr int NZ1 = (NQ == 1) ? 3 : 2;
  constexpr int Z2I = (NQ == 0) ? 0 : (NQ == 1) ? 3 : (NQ == 2) ? 5 : 7;
  constexpr int Z1I = (NQ == 0) ? 0 : (NQ == 1) ? 2 : (NQ == 2) ? 5 : 7;
  constexpr int QI  = (NQ == 0) ? 0 : (NQ == 1) ? 2 : (NQ == 2) ? 3 : 5;
  constexpr int NS = 18;
  const int tid = threadIdx.x, l = tid & 63, g = l >> 4, c16 = l & 15;
  const int qstart = 108 * cg - ((cg & 1) ? 4 : 0);

  const int boff = (64 * sh + c16) * 64 + ((g ^ ((-(c16 >> 2)) & 3)) * 16);
  const int rowp = 16 * (g >> 1) + 4 * (g & 1) + (c16 >> 2);
  const int aoffz = 8192 + rowp * 288 + 8 * (c16 & 3);
  const int aoffq = 8192 + 18432 + rowp * 224 + 8 * (c16 & 3);
  auto rda = [&](const char* slot, int j, int h) -> bf16x4 {
    if (j < NZ2) return lds_tr(slot + aoffz + (Z2I + j) * 32 + h * 8 * 288);
    if (j < NZ2 + NZ1) return lds_tr(slot + aoffz + 9216 + (Z1I + j - NZ2) * 32 + h * 8 * 288);
    return lds_tr(slot + aoffq + (QI + j - NZ2 - NZ1) * 32 + h * 8 * 224);
  };
  auto rd = [&](Frag1<NJ>& f, int t) {
    const char* slot = lds + (t & 3) * K1_SLOT;
#pragma unroll
    for (int m = 0; m < 4; m++) f.b[m] = lds_b128(slot + boff + m * 1024);
#pragma unroll
    for (int j = 0; j < NJ; j++) f.a[j] = cat8(rda(slot, j, 0), rda(slot, j, 1));
  };
  f32x4 acc[NJ][4];
#pragma unroll
  for (int j = 0; j < NJ; j++)
#pragma unroll
    for (int m = 0; m < 4; m++) acc[j][m] = (f32x4){0.f, 0.f, 0.f, 0.f};
  auto step = [&](const Frag1<NJ>& cur, Frag1<NJ>& nxt, int t, bool doissue) {
    const char* slot = lds + ((t + 1) & 3) * K1_SLOT;
    bf16x4 lo[NJ];
#pragma unroll
    for (int x = 0; x < 4 * NJ; x++) {
      const int j = x >> 2, m = x & 3;
      acc[j][m] = mfma(cur.a[j], cur.b[m], acc[j][m]);
      if (x < 4) nxt.b[x] = lds_b128(slot + boff + x * 1024);
      else if (x < 4 + 2 * NJ) {
        const int y = x - 4, jj = y >> 1;
        if ((y & 1) == 0) lo[jj] = rda(slot, jj, 0);
        else nxt.a[jj] = cat8(lo[jj], rda(slot, jj, 1));
      }
      if (doissue && (x & 3) == 1 && (x >> 2) < 5) gs.piece(t + 4, x >> 2, w);
      SB();
    }
  };
  Frag1<NJ> f0, f1;
  wait_vm_lgkm0<15>();
  bar();
  rd(f0, 0);
#pragma unroll 1
  for (int t = 0; t < NS - 4; t += 2) {
    wait_vm_lgkm0<10>(); bar(); SB();
    step(f0, f1, t, true);
    wait_vm_lgkm0<10>(); bar(); SB();
    step(f1, f0, t + 1, true);
  }
  wait_vm_lgkm0<10>(); bar(); SB(); step(f0, f1, NS - 4, false);
  wait_vm_lgkm0<5>();  bar(); SB(); step(f1, f0, NS - 3, false);
  wait_vm_lgkm0<0>();  bar(); SB(); step(f0, f1, NS - 2, false);
#pragma unroll
  for (int j = 0; j < NJ; j++)
#pragma unroll
    for (int m = 0; m < 4; m++) acc[j][m] = mfma(f1.a[j], f1.b[m], acc[j][m]);
  if (w < 4) touch_s((w * 64 + l) * 128, w_sp + (size_t)blockIdx.x * 32768, (unsigned)(uintptr_t)(AS3 char*)lds + K1_PF + w * 256);
  {
    f32x2* st = (f32x2*)(lds + K1_STAT) + NQ * 128 + 64 * sh + c16;
#pragma unroll
    for (int m = 0; m < 4; m++) {
      f32x2 a2 = {0.f, 0.f}, q2 = a2;
#pragma unroll
      for (int j = 0; j < NZ2; j++) {
        const f32x2 u = gelu2(lo2(acc[j][m])), v = gelu2(hi2(acc[j][m]));
        acc[j][m] = __builtin_shufflevector(u, v, 0, 1, 2, 3);
        a2 += u + v;
        q2 = __builtin_elementwise_fma(u, u, __builtin_elementwise_fma(v, v, q2));
      }
      float a = a2[0] + a2[1], q = q2[0] + q2[1];
      a += __shfl_xor(a, 16); q += __shfl_xor(q, 16);
      a += __shfl_xor(a, 32); q += __shfl_xor(q, 32);
      if (g == 0) st[16 * m] = (f32x2){a, q};
    }
  }
  lds_bar();
  AS1 unsigned long long* gstat = (AS1 unsigned long long*)(ws + WS_STAT) + (size_t)rb * 16 * 128;
  if (tid < 128) {
    const f32x2* st = (const f32x2*)(lds + K1_STAT) + tid;
    f32x2 v = st[0] + st[128] + st[256] + st[384];
    const float v0 = v[0], v1 = v[1];
    unsigned lo = __builtin_bit_cast(unsigned, v0), hi = __builtin_bit_cast(unsigned, v1);
    if (hi == 0xffffffffu) hi = 0x7fc00000u;
    __hip_atomic_store(gstat + cg * 128 + tid, ((unsigned long long)hi << 32) | lo, __ATOMIC_RELAXED,
                       __HIP_MEMORY_SCOPE_AGENT);
  }
#pragma unroll
  for (int j = NZ2 + NZ1; j < NJ; j++) {
#pragma unroll
    for (int p = 0; p < 2; p++) {
      const int c = (QI + (j - NZ2 - NZ1)) * 16 + 4 * g + 2 * p + qstart - 108 * cg;
      const int hp = (c * 3641) >> 16, d = c - hp * 18;
      char* dst = lds + K1_QIMG + hp * 4608 + (64 * sh + c16) * 36 + d * 2;
      if (c >= 0 && c < 108) {
#pragma unroll
        for (int m = 0; m < 4; m++) *(unsigned*)(dst + m * 16 * 36) = pk2(acc[j][m][2 * p], acc[j][m][2 * p + 1]);
      }
    }
  }
#pragma unroll
  for (int j = 0; j < NZ1; j++)
#pragma unroll
    for (int mp = 0; mp < 2; mp++) {
      const f32x4 u = acc[NZ2 + j][2 * mp], v = acc[NZ2 + j][2 * mp + 1];
      const u32x4 o = {pk2v(gelu2(lo2(u))), pk2v(gelu2(hi2(u))), pk2v(gelu2(lo2(v))), pk2v(gelu2(hi2(v)))};
      st16(ws + WS_Z1 + ((size_t)(4 * rb + 2 * sh + mp) * 144 + 9 * cg + Z1I + j) * 1024 + l * 16, o);
    }
  const int prow = tid & 127, part = tid >> 7;
  unsigned long long sv[4];
#pragma unroll
  for (int i = 0; i < 4; i++)
    sv[i] = __hip_atomic_load(gstat + (part * 4 + i) * 128 + prow, __ATOMIC_RELAXED, __HIP_MEMORY_SCOPE_AGENT);
  lds_bar();
#pragma unroll
  for (int u = 0; u < 4; u++) {
    const int q = tid + 512 * u;
    if (u < 3 || q < 1728) {
      const int pi = (q * 3641) >> 20, rem = q - pi * 288;
      st16(ws + WS_QKV + ((size_t)(6 * cg + pi) * S_ + rb * 128) * 36 + rem * 16, *(const u32x4*)(lds + K1_QIMG + q * 16));
    }
  }
  {
    float a = 0.f, q = 0.f;
#pragma unroll
    for (int i = 0; i < 4; i++) {
      unsigned long long v = sv[i];
      while ((unsigned)(v >> 32) == 0xffffffffu)
        v = __hip_atomic_load(gstat + (part * 4 + i) * 128 + prow, __ATOMIC_RELAXED, __HIP_MEMORY_SCOPE_AGENT);
      a += __builtin_bit_cast(float, (unsigned)v);
      q += __builtin_bit_cast(float, (unsigned)(v >> 32));
    }
    ((f32x2*)(lds + K1_STAT2))[part * 128 + prow] = (f32x2){a, q};
  }
  lds_bar();
#pragma unroll
  for (int m = 0; m < 4; m++) {
    const f32x2* st = (const f32x2*)(lds + K1_STAT2) + 64 * sh + 16 * m + c16;
    f32x2 v = st[0] + st[128] + st[256] + st[384];
    const float mean = v[0] * (1.0f / FH);
    const float var = fmaxf(v[1] * (1.0f / FH) - mean * mean, 0.f);
    const float rs = 1.0f / __builtin_sqrtf(var + 1e-5f);
    const f32x2 mean2 = {mean, mean}, rs2 = {rs, rs};
    char* dst = lds + K1_ZIMG + (64 * sh + 16 * m + swap23(c16)) * 288 + Z2I * 32 + g * 8;
#pragma unroll
    for (int j = 0; j < NZ2; j++)
      *(u32x2*)(dst + j * 32) = (u32x2){pk2v((lo2(acc[j][m]) - mean2) * rs2), pk2v((hi2(acc[j][m]) - mean2) * rs2)};
  }
  lds_bar();
  {
    char* zt = ws + WS_Z2 + (size_t)cg * 589824 + (size_t)rb * 36864;
#pragma unroll
    for (int u = 0; u < 5; u++) {
      const int q = tid + 512 * u;
      if (u < 4 || q < 2304) *(u32x4*)(zt + q * 16) = *(const u32x4*)(lds + K1_ZIMG + q * 16);
    }
  }
}

__global__ __launch_bounds__(512, 2) void k1_proj(const char* __restrict__ w_in, const char* __restrict__ wqkv,
                                                  char* __restrict__ ws, const char* __restrict__ w_sp) {
  extern __shared__ __attribute__((aligned(16))) char lds[];
  const int b = blockIdx.x, xcd = b & 7, i = b >> 3;
  const int rb = 8 * (xcd & 1) + (i & 7), cg = 4 * (xcd >> 1) + (i >> 3);
  const int w = __builtin_amdgcn_readfirstlane(threadIdx.x >> 6);
  const int sh = w >> 2, nq = sh ? 3 - (w & 3) : (w & 3);
  K1Stage gs;
  gs.init(lds, w_in, wqkv, ws, rb, cg, w);
  gs.issue(0, w); gs.issue(1, w); gs.issue(2, w); gs.issue(3, w);
  if (nq == 0) k1_body<0>(lds, gs, ws, w_sp, rb, cg, w, sh);
  else if (nq == 1) k1_body<1>(lds, gs, ws, w_sp, rb, cg, w, sh);
  else if (nq == 2) k1_body<2>(lds, gs, ws, w_sp, rb, cg, w, sh);
  else k1_body<3>(lds, gs, ws, w_sp, rb, cg, w, sh);
}

struct AttnIn { u32x4 qv[2], k0, k1, v0, v1; unsigned k2, v2; };
DEVI void attn_load(AttnIn& in, const char* __restrict__ ws, int i) {
  const int b = blockIdx.x, h = b & 31, q0 = (b >> 5) * 256;
  const int tid = threadIdx.x, g = (tid & 63) >> 4, c16 = tid & 15;
  const char* qkv = ws + WS_QKV;
  const char* qp = qkv + ((size_t)h * S_ + q0 + 32 * (tid >> 6) + c16) * 36 + (g < 2 ? 16 * g : 20);
  const char* kp = qkv + ((size_t)(32 + h) * S_ + max(q0 - 256 + tid, 0)) * 36;
  const char* vp = kp + (size_t)32 * S_ * 36;
  if (i == 0) in.k0 = *(const u32x4_a4*)kp;
  if (i == 1) in.k1 = *(const u32x4_a4*)(kp + 16);
  if (i == 2) in.k2 = *(const unsigned*)(kp + 32);
  if (i == 3) in.v0 = *(const u32x4_a4*)vp;
  if (i == 4) in.v1 = *(const u32x4_a4*)(vp + 16);
  if (i == 5) in.v2 = *(const unsigned*)(vp + 32);
  if (i == 6) in.qv[0] = *(const u32x4_a4*)qp;
  if (i == 7) in.qv[1] = *(const u32x4_a4*)(qp + 16 * 36);
}

DEVI void attn_image(char* lds, const AttnIn& in) {
  const int tid = threadIdx.x, key = (blockIdx.x >> 5) * 256 - 256 + tid;
  const u32x4 k0 = in.k0, k1 = in.k1, v0 = in.v0, v1 = in.v1;
  const unsigned k2 = in.k2, v2 = in.v2;
  {
    const int f = (-(tid >> 2)) & 3;
    char* kr = lds + tid * 64;
    *(u32x4*)(kr + ((0 ^ f) * 16)) = k0;
    *(u32x4*)(kr + ((1 ^ f) * 16)) = k1;
    *(u32x4*)(kr + ((2 ^ f) * 16)) = (u32x4){k2, key < 0 ? 0x0000f149u : 0u, 0, 0};
    *(u32x4*)(kr + ((3 ^ f) * 16)) = (u32x4){0, 0, 0, 0};
    char* vr = lds + 32768 + tid * 64;
    const int x = ((tid >> 2) & 1) * 32;
    *(u32x4*)(vr + (0 ^ x)) = v0;
    *(u32x4*)(vr + (16 ^ x)) = v1;
    *(u32x4*)(vr + (32 ^ x)) = (u32x4){v2, 0x00003f80u, 0, 0};
    *(u32x4*)(vr + (48 ^ x)) = (u32x4){0, 0, 0, 0};
  }
}

DEVI void attn_phase(char* lds, const AttnIn& in, const char* __restrict__ wo, const char* __restrict__ w_out,
                     char* __restrict__ ws) {
  const int b = blockIdx.x, h = b & 31, qb = b >> 5;
  const int tid = threadIdx.x, l = tid & 63, g = l >> 4, c16 = l & 15;
  const int w = __builtin_amdgcn_readfirstlane(tid >> 6);
  bf16x8 qf[2];
#pragma unroll
  for (int m = 0; m < 2; m++) {
    u32x4 v = in.qv[m];
    if (g >= 2) v = (u32x4){g == 2 ? v[3] : 0u, g == 2 ? 0x00003f80u : 0u, 0, 0};
    qf[m] = __builtin_bit_cast(bf16x8, v);
  }
  const int wu = w * 256 + b;
  WPrep wp;
  if (wu < 810) wp.load(wo, w_out, wu / 9, wu % 9, l);
  const float sc = 0.23570226039551584f * 1.4426950408889634f;
  const int koff = (32 * w + c16) * 64 + ((g ^ ((-(c16 >> 2)) & 3)) * 16);
  const int voff = 32768 + (32 * w + 4 * g + (c16 >> 2)) * 64 + 8 * (c16 & 3);
  const int vx = (g & 1) * 32;
#pragma unroll
  for (int m = 0; m < 2; m++) {
    f32x4 s[17];
#pragma unroll
    for (int i = 0; i < 17; i++) {
      bf16x8 kf = lds_b128(lds + koff + (i + m) * 1024);
      s[i] = mfma(kf, qf[m], (f32x4){0.f, 0.f, 0.f, 0.f});
    }
    const float ninf = -__builtin_inff();
#pragma unroll
    for (int r = 0; r < 4; r++) {
      if (!(c16 < 4 * g + r)) s[0][r] = ninf;
      if (!(4 * g + r <= c16)) s[16][r] = ninf;
    }
    float mxa = ninf, mxb = ninf;
#pragma unroll
    for (int i = 0; i < 17; i++) { mxa = fmx(fmx(mxa, s[i][0]), s[i][1]); mxb = fmx(fmx(mxb, s[i][2]), s[i][3]); }
    float mx = fmx(mxa, mxb);
    mx = fmx(mx, __shfl_xor(mx, 16));
    mx = fmx(mx, __shfl_xor(mx, 32));
    const float nb = -mx * sc;
    const f32x2 sc2 = {sc, sc}, nb2 = {nb, nb};
    unsigned p[18][2];
#pragma unroll
    for (int i = 0; i < 17; i++) {
      const f32x2 ta = __builtin_elementwise_fma((f32x2){s[i][0], s[i][1]}, sc2, nb2);
      const f32x2 tb = __builtin_elementwise_fma((f32x2){s[i][2], s[i][3]}, sc2, nb2);
      const float a0 = ta[0], a1 = ta[1], b0 = tb[0], b1 = tb[1];
      p[i][0] = pk2(__builtin_amdgcn_exp2f(a0), __builtin_amdgcn_exp2f(a1));
      p[i][1] = pk2(__builtin_amdgcn_exp2f(b0), __builtin_amdgcn_exp2f(b1));
    }
    p[17][0] = 0; p[17][1] = 0;
    f32x4 o0 = {0.f, 0.f, 0.f, 0.f}, o1 = o0;
#pragma unroll
    for (int i = 0; i < 9; i++) {
      const int r0 = (2 * i + m) * 1024, r1 = (i < 8) ? r0 + 1024 : r0;
      bf16x8 pf = __builtin_bit_cast(bf16x8, (u32x4){p[2 * i][0], p[2 * i][1], p[2 * i + 1][0], p[2 * i + 1][1]});
      bf16x8 va = cat8(lds_tr(lds + voff + vx + r0), lds_tr(lds + voff + vx + r1));
      bf16x8 vb = cat8(lds_tr(lds + voff + (32 ^ vx) + r0), lds_tr(lds + voff + (32 ^ vx) + r1));
      o0 = mfma(va, pf, o0);
      o1 = mfma(vb, pf, o1);
    }
    const float den = __shfl(o1[2], c16);
    const float inv = 1.0f / den;
    char* op = ws + WS_O + (size_t)(16 * qb + 2 * w + m) * 18432;
    *(u32x2*)(op + (h >> 1) * 1024 + l * 16 + (h & 1) * 8) = (u32x2){pk2(o0[0] * inv, o0[1] * inv), pk2(o0[2] * inv, o0[3] * inv)};
    if (g == 0)
      *(unsigned*)(op + (16 + (h >> 4)) * 1024 + (((h & 7) >> 1) * 16 + c16) * 16 + ((h >> 3) & 1) * 8 + (h & 1) * 4) =
          pk2(o1[0] * inv, o1[1] * inv);
  }
  if (wu < 810) wp.store(ws, wu / 9, wu % 9, l);
}

constexpr int G_SLOT = 16384 + 18432;
constexpr int G_LDS = 4 * G_SLOT;
constexpr int K2_LDS = 81920 + 65536;
template <int NJ> struct Frag2 { bf16x8 a[NJ]; bf16x8 b[4]; };

struct GStage {
  unsigned bo[2], ao[3], bdst, adst;
  const char* Bb;
  const char* Ab;
  DEVI void init(char* lds, const char* B, const char* A, int w) {
    const int l = threadIdx.x & 63;
    Bb = B; Ab = A;
#pragma unroll
    for (int i = 0; i < 2; i++) {
      int r = (2 * w + i) * 8 + (l >> 3);
      int c = (l & 7) ^ ((r >> 1) & 7);
      bo[i] = r * 4096 + c * 16;
    }
#pragma unroll
    for (int i = 0; i < 3; i++) {
      int q = w * 144 + i * 64 + ((i == 2) ? (l & 15) : l);
      ao[i] = q * 16;
    }
    const unsigned lb = (unsigned)(uintptr_t)(AS3 char*)lds;
    bdst = lb + w * 2048; adst = lb + 16384 + w * 2304;
  }
  DEVI void piece(int t, int i) const {
    const unsigned so = (t & 3) * G_SLOT;
    const size_t astep = 18432;
    if (i < 2) glds_s(bo[i], Bb + t * 128, bdst + so + i * 1024);
    else if (i < 4) glds_s(ao[i - 2], Ab + (size_t)t * astep, adst + so + (i - 2) * 1024);
    else glds_s16(ao[2], Ab + (size_t)t * astep, adst + so + 2048);
  }
  DEVI void issue(int t) const {
#pragma unroll
    for (int i = 0; i < 5; i++) piece(t, i);
  }
};

template <int NJ, int NS, int NH, class Hook>
DEVI void gemm_core(char* lds, const GStage& gs, int w, f32x4 (&acc)[NJ][4], Hook hook) {
  const int l = threadIdx.x & 63, g = l >> 4, c16 = l & 15;
  const int fh = w >> 2, th = w & 1, kh = (w >> 1) & 1;
  const int boff = (64 * th + c16) * 128 + (((4 * kh + g) ^ (c16 >> 1)) * 16);
  const int aoff = 16384 + (32 * kh + 16 * (g >> 1) + 4 * (g & 1) + (c16 >> 2)) * 288 + fh * 160 + 8 * (c16 & 3);
  auto rd = [&](Frag2<NJ>& f, int t) {
    const char* slot = lds + (t & 3) * G_SLOT;
#pragma unroll
    for (int m = 0; m < 4; m++) f.b[m] = lds_b128(slot + boff + m * 2048);
#pragma unroll
    for (int j = 0; j < NJ; j++)
      f.a[j] = cat8(lds_tr(slot + aoff + j * 32), lds_tr(slot + aoff + j * 32 + 8 * 288));
  };
  auto step = [&](const Frag2<NJ>& cur, Frag2<NJ>& nxt, int t, bool doissue, int hb = -1) {
    const char* slot = lds + ((t + 1) & 3) * G_SLOT;
    const char* pb = slot + boff;
    const char* pa = slot + aoff;
    bf16x4 lo[NJ];
#pragma unroll
    for (int x = 0; x < 4 * NJ; x++) {
      const int j = x >> 2, m = x & 3;
      acc[j][m] = mfma(cur.a[j], cur.b[m], acc[j][m]);
      if (x < 4) nxt.b[x] = lds_b128(pb + x * 2048);
      else if (x < 4 + 2 * NJ) {
        const int y = x - 4, jj = y >> 1;
        if ((y & 1) == 0) lo[jj] = lds_tr(pa + jj * 32);
        else nxt.a[jj] = cat8(lo[jj], lds_tr(pa + jj * 32 + 8 * 288));
      }
      if (doissue && (x % 3) == 1 && x / 3 < 5) gs.piece(t + 4, x / 3);
      if (hb >= 0 && (x % 3) == 1 && x / 3 < 5 && hb + x / 3 < NH) hook(hb + x / 3);
      SB();
    }
  };
  auto mm = [&](const Frag2<NJ>& f) {
#pragma unroll
    for (int j = 0; j < NJ; j++)
#pragma unroll
      for (int m = 0; m < 4; m++) acc[j][m] = mfma(f.a[j], f.b[m], acc[j][m]);
  };
#pragma unroll
  for (int j = 0; j < NJ; j++)
#pragma unroll
    for (int m = 0; m < 4; m++) acc[j][m] = (f32x4){0.f, 0.f, 0.f, 0.f};
  Frag2<NJ> f0, f1;
  wait_vm_lgkm0<15>();
  bar();
  rd(f0, 0);
  constexpr int M = NS - 4, MP = M & ~1;
#pragma unroll 1
  for (int t = 0; t < MP; t += 2) {
    wait_vm_lgkm0<10>(); bar(); SB();
    step(f0, f1, t, true);
    wait_vm_lgkm0<10>(); bar(); SB();
    step(f1, f0, t + 1, true);
  }
  wait_vm_lgkm0<10>(); bar(); SB(); step(f0, f1, NS - 4, false, 0);
  wait_vm_lgkm0<10>(); bar(); SB(); step(f1, f0, NS - 3, false, 5);
  wait_vm_lgkm0<10>(); bar(); SB(); step(f0, f1, NS - 2, false, 10);
  mm(f1);
}

template <int NJ>
DEVI void khalf_exchange(char* lds, int w, f32x4 (&acc)[NJ][4]) {
  const int l = threadIdx.x & 63, kh = (w >> 1) & 1;
  lds_bar();
  f32x4* mine = (f32x4*)(lds + w * 10240) + l;
  const f32x4* other = (const f32x4*)(lds + (w ^ 2) * 10240) + l;
  if (kh == 0) {
#pragma unroll
    for (int j = 0; j < NJ; j++) { mine[(2 * j) * 64] = acc[j][2]; mine[(2 * j + 1) * 64] = acc[j][3]; }
  } else {
#pragma unroll
    for (int j = 0; j < NJ; j++) {
      mine[(2 * j) * 64] = acc[j][0]; mine[(2 * j + 1) * 64] = acc[j][1];
      acc[j][0] = acc[j][2]; acc[j][1] = acc[j][3];
    }
  }
  lds_bar();
#pragma unroll
  for (int j = 0; j < NJ; j++) { acc[j][0] += other[(2 * j) * 64]; acc[j][1] += other[(2 * j + 1) * 64]; }
}

template <int NJ>
DEVI void k2_body(char* lds, const GStage& gs, const char* __restrict__ b_sp, char* __restrict__ ws,
                  int tb, int fb, int w, AttnIn& ain) {
  f32x4 acc[NJ][4];
  const int l = threadIdx.x & 63, c16 = l & 15;
  const int fh = w >> 2, th = w & 1, kh = (w >> 1) & 1;
  const int trow = tb * 128 + 64 * th + 32 * kh + c16;
  u32x4 z[NJ];
  unsigned bsr[2];
  gemm_core<NJ, 32, 10 + NJ>(lds, gs, w, acc, [&](int i) {
    if (i < 8) attn_load(ain, ws, i);
    else if (i < 10) bsr[i - 8] = *(const unsigned short*)(b_sp + (trow + 16 * (i - 8)) * 2);
    else z[i - 10] = *(const u32x4*)(ws + WS_Z1 + ((size_t)(4 * tb + 2 * th + kh) * 144 + 9 * fb + 5 * fh + (i - 10)) * 1024 + l * 16);
  });
  khalf_exchange<NJ>(lds, w, acc);
  const float bs[2] = {__builtin_bit_cast(float, bsr[0] << 16), __builtin_bit_cast(float, bsr[1] << 16)};
  const int J0 = 9 * fb + 5 * fh;
#pragma unroll
  for (int m = 0; m < 2; m++) {
    u32x2 o[NJ];
#pragma unroll
    for (int j = 0; j < NJ; j++) {
      const f32x4 a = acc[j][m];
      const unsigned z0 = z[j][2 * m], z1 = z[j][2 * m + 1];
      o[j] = (u32x2){pk2(bflo(z0) * (a[0] + bs[m]), bfhi(z0) * (a[1] + bs[m])),
                     pk2(bflo(z1) * (a[2] + bs[m]), bfhi(z1) * (a[3] + bs[m]))};
    }
    char* hp = ws + WS_H + (size_t)(8 * tb + 4 * th + 2 * kh + m) * 73728 + (J0 >> 1) * 1024 + l * 16;
    if (J0 & 1) {
      *(u32x2*)(hp + 8) = o[0];
      st16(hp + 1024, (u32x4){o[1][0], o[1][1], o[2][0], o[2][1]});
      if constexpr (NJ == 5) st16(hp + 2048, (u32x4){o[3][0], o[3][1], o[4][0], o[4][1]});
      else *(u32x2*)(hp + 2048) = o[3];
    } else {
      st16(hp, (u32x4){o[0][0], o[0][1], o[1][0], o[1][1]});
      st16(hp + 1024, (u32x4){o[2][0], o[2][1], o[3][0], o[3][1]});
      if constexpr (NJ == 5) *(u32x2*)(hp + 2048) = o[4];
    }
  }
}
__global__ __launch_bounds__(512, 2) void k2_sgu(const char* __restrict__ w_sp, const char* __restrict__ b_sp,
                                                 const char* __restrict__ wo, const char* __restrict__ w_out,
                                                 char* __restrict__ ws) {
  extern __shared__ __attribute__((aligned(16))) char lds[];
  const int b = blockIdx.x, xcd = b & 7, i = b >> 3;
  const int tb = 8 * (xcd & 1) + (i & 7), fb = 4 * (xcd >> 1) + (i >> 3);
  const int w = __builtin_amdgcn_readfirstlane(threadIdx.x >> 6);
  GStage gs;
  gs.init(lds, w_sp + (size_t)tb * 128 * 4096, ws + WS_Z2 + (size_t)fb * 589824, w);
  gs.issue(0); gs.issue(1); gs.issue(2); gs.issue(3);
  AttnIn ain;
  if (w < 4) k2_body<5>(lds, gs, b_sp, ws, tb, fb, w, ain);
  else k2_body<4>(lds, gs, b_sp, ws, tb, fb, w, ain);
  attn_image(lds + 81920, ain);
  lds_bar();
  attn_phase(lds + 81920, ain, wo, w_out, ws);
}

constexpr int KC_LDS = 8 * 20 * 1024;
struct FragC { u32x4 h[5]; u32x4 wv[4]; };

DEVI void kc_prio(int p) {
  if (p == 3) __builtin_amdgcn_s_setprio(3);
  else if (p == 2) __builtin_amdgcn_s_setprio(2);
  else if (p == 1) __builtin_amdgcn_s_setprio(1);
  else __builtin_amdgcn_s_setprio(0);
}
template <int NB, int KSTEP>
DEVI void kc_stream(const char* __restrict__ hb, const unsigned (&ho)[5], const char* __restrict__ wb, unsigned wofs,
                    f32x4 (&acc)[5][4]) {
  constexpr int NBUF = 3;
  auto ld = [&](FragC& f, int i) {
    const int ii = i;
#pragma unroll
    for (int m = 0; m < 5; m++) f.h[m] = *(const u32x4*)(hb + (size_t)ii * (KSTEP * 1024) + ho[m]);
#pragma unroll
    for (int j = 0; j < 4; j++) f.wv[j] = *(const u32x4*)(wb + (size_t)ii * (KSTEP * 36 * 1024) + wofs + j * 1024);
    SB();
  };
  auto mm = [&](const FragC& f) {
#pragma unroll
    for (int m = 0; m < 5; m++)
#pragma unroll
      for (int j = 0; j < 4; j++)
        acc[m][j] = mfma(__builtin_bit_cast(bf16x8, f.wv[j]), __builtin_bit_cast(bf16x8, f.h[m]), acc[m][j]);
    SB();
  };
  FragC f[NBUF];
#pragma unroll
  for (int i = 0; i < NBUF - 1; i++) ld(f[i], i);
#pragma unroll
  for (int i = 0; i < NB; i++) {
    if (i == 0 || (4 * i) / NB != (4 * (i - 1)) / NB) kc_prio(3 - (4 * i) / NB);
    if (i + NBUF - 1 < NB) ld(f[(i + NBUF - 1) % NBUF], i + NBUF - 1);
    mm(f[i % NBUF]);
  }
}

__global__ __launch_bounds__(512, 2) void kc_out(const char* __restrict__ x, char* __restrict__ ws,
                                                 char* __restrict__ out) {
  extern __shared__ __attribute__((aligned(16))) char lds[];
  const int b = blockIdx.x, xcd = b & 7, i = b >> 3;
  const int unit = xcd * 30 + i;
  if (unit >= 234) return;
  const int tb = unit / 9, db = unit - tb * 9;
  const int l = threadIdx.x & 63, g = l >> 4, c16 = l & 15;
  const int w = __builtin_amdgcn_readfirstlane(threadIdx.x >> 6);
  f32x4 acc[5][4];
#pragma unroll
  for (int m = 0; m < 5; m++)
#pragma unroll
    for (int j = 0; j < 4; j++) acc[m][j] = (f32x4){0.f, 0.f, 0.f, 0.f};
  unsigned ho[5];
  if (w < 6) {
#pragma unroll
    for (int m = 0; m < 5; m++) ho[m] = min(5 * tb + m, 127) * 73728 + l * 16;
    kc_stream<12, 6>(ws + WS_H + w * 1024, ho, ws + WS_WF + (size_t)(w * 36 + db * 4) * 1024, l * 16, acc);
  } else {
#pragma unroll
    for (int m = 0; m < 5; m++) ho[m] = min(5 * tb + m, 127) * 18432 + l * 16;
    kc_stream<9, 2>(ws + WS_O + (w - 6) * 1024, ho, ws + WS_WF + (size_t)((72 + w - 6) * 36 + db * 4) * 1024, l * 16, acc);
  }
  u32x2 xv[3];
#pragma unroll
  for (int q = 0; q < 3; q++) {
    const int st = min(w + 8 * q, 19), ts = min(5 * tb + (st >> 2), 127);
    xv[q] = *(const u32x2*)(x + (size_t)(ts * 16 + c16) * 1152 + (db * 64 + (st & 3) * 16 + 4 * g) * 2);
  }
  f32x4* red = (f32x4*)lds;
#pragma unroll
  for (int m = 0; m < 5; m++)
#pragma unroll
    for (int j = 0; j < 4; j++) red[(w * 20 + m * 4 + j) * 64 + l] = acc[m][j];
  asm volatile("s_waitcnt lgkmcnt(0)" ::: "memory");
  lds_bar();
#pragma unroll
  for (int q = 0; q < 3; q++) {
    const int st = w + 8 * q, ts = 5 * tb + (st >> 2);
    if (st < 20 && ts < 128) {
      const f32x4* p = red + st * 64 + l;
      f32x4 y = (p[0] + p[20 * 64]) + (p[40 * 64] + p[60 * 64]) + (p[80 * 64] + p[100 * 64]);
      f32x4 sv = p[120 * 64] + p[140 * 64];
      const float xf[4] = {bflo(xv[q][0]), bfhi(xv[q][0]), bflo(xv[q][1]), bfhi(xv[q][1])};
      float o[4];
#pragma unroll
      for (int e = 0; e < 4; e++) {
        const float sig = __builtin_amdgcn_rcpf(1.0f + __builtin_amdgcn_exp2f(-1.4426950408889634f * y[e]));
        o[e] = xf[e] + sv[e] * (y[e] * sig);
      }
      *(u32x2*)(out + (size_t)(ts * 16 + c16) * 1152 + (db * 64 + (st & 3) * 16 + 4 * g) * 2) =
          (u32x2){pk2(o[0], o[1]), pk2(o[2], o[3])};
    }
  }
}

extern "C" void kernel_launch(void* const* d_in, const int* in_sizes, int n_in, void* d_out, int out_size,
                              void* d_ws, size_t ws_size, hipStream_t stream) {
  const char* x = (const char*)d_in[0];
  const char* wqkv = (const char*)d_in[1];
  const char* wo = (const char*)d_in[2];
  const char* w_in = (const char*)d_in[4];
  const char* w_sp = (const char*)d_in[5];
  const char* b_sp = (const char*)d_in[6];
  const char* w_out = (const char*)d_in[7];
  char* ws = (char*)d_ws;
  (void)hipFuncSetAttribute((const void*)k1_proj, hipFuncAttributeMaxDynamicSharedMemorySize, K1_LDS);
  (void)hipFuncSetAttribute((const void*)k2_sgu, hipFuncAttributeMaxDynamicSharedMemorySize, K2_LDS);
  (void)hipFuncSetAttribute((const void*)kc_out, hipFuncAttributeMaxDynamicSharedMemorySize, KC_LDS);
  constexpr int NPF = 57344 / 256;
  k0_ln<<<256 + NPF, 64, 0, stream>>>(x, ws, w_in, wqkv);
  k1_proj<<<256, 512, K1_LDS, stream>>>(w_in, wqkv, ws, w_sp);
  k2_sgu<<<256, 512, K2_LDS, stream>>>(w_sp, b_sp, wo, w_out, ws);
  kc_out<<<8 * 30, 512, KC_LDS, stream>>>(x, ws, (char*)d_out);
}
```
